```python
import torch

def _softmax(x, axis=-1):
    return torch.softmax(x, dim=axis)
def _gelu(x):
    return torch.nn.functional.gelu(x, approximate="tanh")
def _silu(x):
    return torch.nn.functional.silu(x)
def _sigmoid(x):
    return torch.sigmoid(x)
def _layernorm(x, axis=-1, eps=1e-5):
    m = x.mean(dim=axis, keepdim=True)
    v = x.var(dim=axis, keepdim=True, unbiased=False)
    return (x - m) / torch.sqrt(v + eps)
def _scatter_add(tgt, idx, src, axis=0):
    return tgt.clone().index_add_(axis, idx, src)
def _segment_sum(src, seg, n):
    out = torch.zeros((n,) + src.shape[1:], dtype=src.dtype)
    return out.index_add_(0, seg, src)
def _pad(x, pw):
    flat = tuple(v for pair in reversed(pw) for v in pair)
    return torch.nn.functional.pad(x, flat)

kernel_name = 'parallel_sum__ssm_conv_h3__dense_activation_silu__prompt'

def setup_inputs():

    _w = (lambda a: (lambda t: t.to(torch.bfloat16) if t.is_floating_point() else t)(torch.as_tensor(a)))
    def _A_setup():
        B, S, D = 1, 2048, 1024
        ks = 4
        return {
            "q": _w((lambda *s: torch.randn(*s, dtype=torch.bfloat16) * 0.05)(B, S, D)),
            "k": _w((lambda *s: torch.randn(*s, dtype=torch.bfloat16) * 0.2)(B, S, D)),
            "v": _w((lambda *s: torch.randn(*s, dtype=torch.bfloat16) * 0.2)(B, S, D)),
            "shift_k": _w((lambda *s: torch.randn(*s, dtype=torch.bfloat16) * 0.2)(D, ks) * 0.5),
            "ssm_k": _w((lambda *s: torch.randn(*s, dtype=torch.bfloat16) * 0.4)(D, S) * (1.0 / S ** 0.5)),
            "ssm_D": _w((lambda *s: torch.randn(*s, dtype=torch.bfloat16) * 0.2)(D)),
        }
    _A_in = _A_setup()

    def _B_setup():
        B, S, D, F = 1, 2048, 1024, 3584
        return {
            "x": _w((lambda *s: torch.randn(*s, dtype=torch.bfloat16) * 0.05)(B, S, D)),
            "w1": _w((lambda *s: torch.randn(*s, dtype=torch.bfloat16) * 0.8)(D, F) * (D ** -0.5)),
            "w2": _w((lambda *s: torch.randn(*s, dtype=torch.bfloat16) * 0.1)(F, D) * (F ** -0.5)),
        }
    _B_in = _B_setup()

    return {
        "A_q": _A_in["q"],
        "A_k": _A_in["k"],
        "A_v": _A_in["v"],
        "A_shift_k": _A_in["shift_k"],
        "A_ssm_k": _A_in["ssm_k"],
        "A_ssm_D": _A_in["ssm_D"],
        "B_x": _B_in["x"],
        "B_w1": _B_in["w1"],
        "B_w2": _B_in["w2"],
    }

def reference(A_q, A_k, A_v, A_shift_k, A_ssm_k, A_ssm_D, B_x, B_w1, B_w2):
    def _A_ref(q, k, v, shift_k, ssm_k, ssm_D):
        B, S, D = q.shape
        ks = shift_k.shape[-1]
        k_pad = _pad(k, ((0, 0), (ks - 1, 0), (0, 0)))
        k_shift = torch.zeros_like(k)
        for t in range(ks):
            k_shift = k_shift + k_pad[:, t:t + S, :] * shift_k[:, ks - 1 - t][None, None, :]
        kv = k_shift * v
        L = 2 * S
        kv_pad = _pad(kv, ((0, 0), (0, S), (0, 0)))
        kf_pad = _pad(ssm_k, ((0, 0), (0, S)))
        kv_f = (lambda x, axes=(-2,-1): torch.fft.rfft2(x.float(), dim=axes))(kv_pad[:, None, :, :], axes=(1, 2))
        kf_f = (lambda x, axes=(-2,-1): torch.fft.rfft2(x.float(), dim=axes))(kf_pad[:, None, :], axes=(1, 2))
        kf_f = (lambda x, *d: x.permute(*d))(kf_f, 1, 2, 0)
        y_f = kv_f * kf_f[None, :, :, :]
        y = (lambda x, s, axes=(-2,-1): torch.fft.irfft2(x, s=s, dim=axes).to(torch.bfloat16))(y_f, (1, L), axes=(1, 2))[:, 0, :S, :]
        y = y + kv * ssm_D[None, None, :]
        return q * y

    def _B_ref(x, w1, w2):
        h = torch.einsum("bsd,df->bsf", x, w1)
        h = _silu(h)
        out = torch.einsum("bsf,fd->bsd", h, w2)
        return out

    x = A_q
    xn = _layernorm(x, axis=-1)
    return x + _A_ref(xn, A_k, A_v, A_shift_k, A_ssm_k, A_ssm_D) + _B_ref(xn, B_w1, B_w2)
```

```cpp
#include <hip/hip_runtime.h>
#include <hip/hip_bf16.h>
typedef unsigned short u16;
constexpr int S=2048, D=1024, F=3584;
__device__ __forceinline__ float bf2f(u16 v){ return __uint_as_float(((unsigned)v)<<16); }
__device__ __forceinline__ u16 f2bf(float f){ unsigned u=__float_as_uint(f); u += 0x7fff + ((u>>16)&1); return (u16)(u>>16); }
__device__ __forceinline__ float rbf(float f){ return bf2f(f2bf(f)); }

__global__ void k_ln(const u16* x, u16* xn){
  int row=blockIdx.x; int t=threadIdx.x;
  __shared__ float red[256];
  float v[4]; float s=0;
  for(int i=0;i<4;i++){ v[i]=bf2f(x[row*D+t*4+i]); s+=v[i]; }
  red[t]=s; __syncthreads();
  for(int o=128;o>0;o>>=1){ if(t<o) red[t]+=red[t+o]; __syncthreads(); }
  float m=red[0]/D; __syncthreads();
  float q=0; for(int i=0;i<4;i++){ float d=v[i]-m; q+=d*d; }
  red[t]=q; __syncthreads();
  for(int o=128;o>0;o>>=1){ if(t<o) red[t]+=red[t+o]; __syncthreads(); }
  float var=red[0]/D;
  float mb=rbf(m), vb=rbf(var);
  float sd=rbf(sqrtf(rbf(vb+1e-5f)));
  for(int i=0;i<4;i++) xn[row*D+t*4+i]=f2bf(rbf(v[i]-mb)/sd);
}
__global__ void k_kv(const u16* k,const u16* v,const u16* sk,u16* kv){
  int idx=blockIdx.x*256+threadIdx.x; int s=idx/D, d=idx%D;
  float acc=0;
  for(int t=0;t<4;t++){ int ss=s+t-3; float kk= ss>=0? bf2f(k[ss*D+d]):0.f; acc=rbf(acc+rbf(kk*bf2f(sk[d*4+3-t]))); }
  kv[idx]=f2bf(acc*bf2f(v[idx]));
}
__global__ void k_conv(const u16* kv,const u16* h,const u16* Dv,u16* y){
  int d=blockIdx.x; __shared__ float u[S], hh[S];
  for(int i=threadIdx.x;i<S;i+=256){ u[i]=bf2f(kv[i*D+d]); hh[i]=bf2f(h[d*S+i]); }
  __syncthreads();
  for(int s=threadIdx.x;s<S;s+=256){ float a=0; for(int j=0;j<=s;j++) a+=hh[j]*u[s-j];
    y[s*D+d]=f2bf(rbf(a)+rbf(u[s]*bf2f(Dv[d]))); }
}
template<int MODE>
__global__ void k_gemm(const u16* A,const u16* B,u16* C,int M,int N,int K,const u16* x,const u16* xn,const u16* y){
  __shared__ float sa[16][65], sb[16][65];
  int tx=threadIdx.x%16, ty=threadIdx.x/16; int m0=blockIdx.y*64, n0=blockIdx.x*64;
  float acc[4][4]={};
  for(int k0=0;k0<K;k0+=16){
    for(int i=threadIdx.x;i<1024;i+=256){ int r=i/16,c=i%16; sa[c][r]=bf2f(A[(m0+r)*K+k0+c]); int kr=i/64, cc=i%64; sb[kr][cc]=bf2f(B[(k0+kr)*N+n0+cc]); }
    __syncthreads();
    for(int k=0;k<16;k++) for(int i=0;i<4;i++) for(int j=0;j<4;j++) acc[i][j]+=sa[k][ty*4+i]*sb[k][tx*4+j];
    __syncthreads();
  }
  for(int i=0;i<4;i++) for(int j=0;j<4;j++){ int r=m0+ty*4+i,c=n0+tx*4+j; float a=rbf(acc[i][j]);
    if(MODE==0){ C[r*N+c]=f2bf(a/(1.f+__expf(-a))); }
    else { float t=rbf(bf2f(x[r*N+c])+rbf(bf2f(xn[r*N+c])*bf2f(y[r*N+c]))); C[r*N+c]=f2bf(t+a); } }
}
extern "C" void kernel_launch(void* const* d_in, const int* in_sizes, int n_in, void* d_out, int out_size, void* d_ws, size_t ws_size, hipStream_t stream){
  const u16* x=(const u16*)d_in[0]; const u16* k=(const u16*)d_in[1]; const u16* v=(const u16*)d_in[2];
  const u16* sk=(const u16*)d_in[3]; const u16* h=(const u16*)d_in[4]; const u16* Dv=(const u16*)d_in[5];
  const u16* w1=(const u16*)d_in[7]; const u16* w2=(const u16*)d_in[8];
  u16* ws=(u16*)d_ws; u16* xn=ws; u16* kv=xn+S*D; u16* y=kv+S*D; u16* H=y+S*D;
  k_ln<<<S,256,0,stream>>>(x,xn);
  k_kv<<<S*D/256,256,0,stream>>>(k,v,sk,kv);
  k_conv<<<D,256,0,stream>>>(kv,h,Dv,y);
  k_gemm<0><<<dim3(F/64,S/64),256,0,stream>>>(xn,w1,H,S,F,D,nullptr,nullptr,nullptr);
  k_gemm<1><<<dim3(D/64,S/64),256,0,stream>>>(H,w2,(u16*)d_out,S,D,F,x,xn,y);
}
```

```cpp
#include <hip/hip_runtime.h>
#include <hip/hip_bf16.h>

#define LAS __attribute__((address_space(3)))
#define GAS __attribute__((address_space(1)))
#define DI __device__ __forceinline__
typedef unsigned short u16;
typedef short s16x4 __attribute__((ext_vector_type(4)));
typedef short s16x8 __attribute__((ext_vector_type(8)));
typedef __bf16 bf16x8 __attribute__((ext_vector_type(8)));
typedef __bf16 bf16x2 __attribute__((ext_vector_type(2)));
typedef float f32x4 __attribute__((ext_vector_type(4)));
typedef float f32x2 __attribute__((ext_vector_type(2)));
typedef unsigned u32x4 __attribute__((ext_vector_type(4)));
typedef unsigned u32x2 __attribute__((ext_vector_type(2)));

constexpr int S = 2048, D = 1024, F = 3584;
constexpr unsigned WS_FLAGS = 0;
constexpr unsigned WS_XN = 65536;
constexpr unsigned WS_U = WS_XN + S * D * 2;
constexpr unsigned WS_Y = WS_U + S * D * 2;
constexpr unsigned WS_H = WS_Y + S * D * 2;
constexpr unsigned WS_P = WS_H + S * F * 2;
constexpr unsigned WS_TS = WS_P + S * D * 4;
constexpr unsigned FL_H = 0, FL_Y = 32 * 256, FL_P = 16384;
constexpr int XO = 0, WFO = 49152, WHO = 122880, SPO = 135168, LDS_BYTES = 163840;
constexpr int HIMG = 4224;
constexpr int UPAD = 640;
constexpr int A2O = 0, B2O = 65536;

#define LEAD 2
#define GM 8
#define STAMP 0

#if defined(__HIP_DEVICE_COMPILE__)
#define RLX_AGENT __ATOMIC_RELAXED, __HIP_MEMORY_SCOPE_AGENT
#define BAR() do { asm volatile("" ::: "memory"); __builtin_amdgcn_s_barrier(); asm volatile("" ::: "memory"); } while (0)
#define WAITV(n) asm volatile("s_waitcnt vmcnt(" #n ")" ::: "memory")
#define WAITL0() asm volatile("s_waitcnt lgkmcnt(0)" ::: "memory")
#define SCHED() __builtin_amdgcn_sched_barrier(0)
#define MFMA(a, b, c) __builtin_amdgcn_mfma_f32_16x16x32_bf16((a), (b), (c), 0, 0, 0)
DI float bflo(unsigned u) { return __uint_as_float(u << 16); }
DI float bfhi(unsigned u) { return __uint_as_float(u & 0xffff0000u); }
DI unsigned pk(float a, float b) { f32x2 v = {a, b}; return __builtin_bit_cast(unsigned, __builtin_convertvector(v, bf16x2)); }
DI float rbf(float a) { return bflo(pk(a, a)); }
DI float silu(float t) { return t * __builtin_amdgcn_rcpf(1.f + __builtin_amdgcn_exp2f(t * -1.44269504f)); }
DI bf16x8 ldA(LAS char* p) { return *(LAS bf16x8*)p; }
DI bf16x8 ldT(LAS char* p, int off2) {
  s16x4 a = __builtin_amdgcn_ds_read_tr16_b64_v4i16((LAS s16x4*)p);
  s16x4 b = __builtin_amdgcn_ds_read_tr16_b64_v4i16((LAS s16x4*)(p + off2));
  return __builtin_bit_cast(bf16x8, (s16x8)__builtin_shufflevector(a, b, 0, 1, 2, 3, 4, 5, 6, 7));
}
DI u32x4 mksrd(const void* p, unsigned bytes) {
  unsigned long long a = (unsigned long long)p;
  return (u32x4){(unsigned)a, (unsigned)(a >> 32) & 0xffffu, bytes, 0x00020000u};
}
DI void dma(unsigned lds, unsigned voff, u32x4 srd, unsigned soff) {
  asm volatile("s_mov_b32 m0, %0\n\ts_nop 0\n\tbuffer_load_dwordx4 %1, %2, %3 offen lds" ::"s"(lds), "v"(voff), "s"(srd), "s"(soff) : "memory");
}
DI void dma_sc1(unsigned lds, unsigned voff, u32x4 srd, unsigned soff) {
  asm volatile("s_mov_b32 m0, %0\n\ts_nop 0\n\tbuffer_load_dwordx4 %1, %2, %3 offen sc1 lds" ::"s"(lds), "v"(voff), "s"(srd), "s"(soff) : "memory");
}
DI void dma4_sc1(unsigned lds, unsigned voff, u32x4 srd, unsigned soff) {
  asm volatile("s_mov_b32 m0, %0\n\ts_nop 0\n\tbuffer_load_dwordx4 %1, %2, %3 offen sc1 lds\n\tbuffer_load_dwordx4 %1, %2, %3 offen offset:1024 sc1 lds\n\t"
               "buffer_load_dwordx4 %1, %2, %3 offen offset:2048 sc1 lds\n\tbuffer_load_dwordx4 %1, %2, %3 offen offset:3072 sc1 lds" ::"s"(lds), "v"(voff), "s"(srd), "s"(soff) : "memory");
}
DI void dma4(unsigned lds, unsigned voff, u32x4 srd, unsigned soff) {
  asm volatile("s_mov_b32 m0, %0\n\ts_nop 0\n\tbuffer_load_dwordx4 %1, %2, %3 offen lds\n\tbuffer_load_dwordx4 %1, %2, %3 offen offset:1024 lds\n\t"
               "buffer_load_dwordx4 %1, %2, %3 offen offset:2048 lds\n\tbuffer_load_dwordx4 %1, %2, %3 offen offset:3072 lds" ::"s"(lds), "v"(voff), "s"(srd), "s"(soff) : "memory");
}
DI void wait_ge(GAS unsigned* w, unsigned want) {
#pragma unroll 1
  for (unsigned spins = 0; spins < (1u << 18); ++spins) {
    if ((unsigned)__builtin_amdgcn_readfirstlane(__hip_atomic_load(w, RLX_AGENT)) >= want) break;
    __builtin_amdgcn_s_sleep(1);
  }
  asm volatile("" ::: "memory");
}
DI int lane_id() { int l; asm volatile("v_mbcnt_lo_u32_b32 %0, -1, 0\n\tv_mbcnt_hi_u32_b32 %0, -1, %0" : "=v"(l)); __builtin_assume(l >= 0 && l < 64); return l; }
DI float wsum(float v) {
#pragma unroll
  for (int o = 32; o > 0; o >>= 1) v += __shfl_xor(v, o);
  return v;
}
DI int kperm(int t, int ph, int n) {
  if (LEAD == 0) return t;
  const int r = (t - ph) & 3, base = t - r;
  const int d = r == 0 ? LEAD : (r <= LEAD ? -1 : 0);
  return t + ((base < 0 || base + LEAD >= n) ? 0 : d);
}
#define TS(i)
#define TSOUT(n)
#endif

__global__ __launch_bounds__(512) void k_prep(const u16* x, const u16* k, const u16* v, const u16* sk, char* ws) {
#if defined(__HIP_DEVICE_COMPILE__)
  __shared__ __attribute__((aligned(16))) char img_[16384];
  LAS char* img = (LAS char*)img_;
  const int t = threadIdx.x, wid = t >> 6, lane = t & 63, b = blockIdx.x;
  if (t < 16) *(u32x4*)(ws + WS_FLAGS + b * 256 + t * 16) = (u32x4){0, 0, 0, 0};
  const int s0 = (b >> 4) * 128, d0 = (b & 15) * 64, c = t & 7, r0 = (t >> 3) * 2;
  u32x4 kk[5], vv[2], w4[4];
#pragma unroll
  for (int i = 0; i < 5; ++i) { int s = s0 + r0 - 3 + i; kk[i] = s >= 0 ? *(const u32x4*)(k + s * D + d0 + c * 8) : (u32x4){0, 0, 0, 0}; }
#pragma unroll
  for (int i = 0; i < 2; ++i) vv[i] = *(const u32x4*)(v + (s0 + r0 + i) * D + d0 + c * 8);
#pragma unroll
  for (int i = 0; i < 4; ++i) w4[i] = *(const u32x4*)(sk + (d0 + c * 8) * 4 + i * 8);
  const int row = b * 8 + wid;
  u32x4 xa = *(const u32x4*)(x + row * D + lane * 8), xb = *(const u32x4*)(x + row * D + 512 + lane * 8);
#pragma unroll
  for (int i = 0; i < 2; ++i) {
    u32x4 o;
#pragma unroll
    for (int e = 0; e < 4; ++e) {
      float lo = bflo(kk[i + 3][e]) * bflo(w4[e][0]) + bflo(kk[i + 2][e]) * bfhi(w4[e][0]) + bflo(kk[i + 1][e]) * bflo(w4[e][1]) + bflo(kk[i][e]) * bfhi(w4[e][1]);
      float hi = bfhi(kk[i + 3][e]) * bflo(w4[e][2]) + bfhi(kk[i + 2][e]) * bfhi(w4[e][2]) + bfhi(kk[i + 1][e]) * bflo(w4[e][3]) + bfhi(kk[i][e]) * bfhi(w4[e][3]);
      o[e] = pk(lo * bflo(vv[i][e]), hi * bfhi(vv[i][e]));
    }
    int r = r0 + i, swz = ((r >> 1) & 1) | (((r >> 3) & 1) << 1);
    *(LAS u32x4*)(img + r * 128 + ((c ^ (swz << 1)) << 4)) = o;
  }
  float f[16], sum = 0.f;
#pragma unroll
  for (int e = 0; e < 4; ++e) { f[2 * e] = bflo(xa[e]); f[2 * e + 1] = bfhi(xa[e]); f[8 + 2 * e] = bflo(xb[e]); f[9 + 2 * e] = bfhi(xb[e]); }
#pragma unroll
  for (int e = 0; e < 16; ++e) sum += f[e];
  const float mean = wsum(sum) * (1.f / D);
  float q2 = 0.f;
#pragma unroll
  for (int e = 0; e < 16; ++e) { float d = f[e] - mean; q2 += d * d; }
  const float var = wsum(q2) * (1.f / D);
  const float rs = 1.f / sqrtf(var + 1e-5f);
  u32x4 oa, ob;
#pragma unroll
  for (int e = 0; e < 4; ++e) { oa[e] = pk((f[2 * e] - mean) * rs, (f[2 * e + 1] - mean) * rs); ob[e] = pk((f[8 + 2 * e] - mean) * rs, (f[9 + 2 * e] - mean) * rs); }
  *(u32x4*)(ws + WS_XN + (row * D + lane * 8) * 2) = oa;
  *(u32x4*)(ws + WS_XN + (row * D + 512 + lane * 8) * 2) = ob;
  __syncthreads();
  const int g = lane >> 4, q = (lane & 15) >> 2, p = lane & 3, swz = ((q >> 1) & 1) | ((g & 1) << 1);
#pragma unroll
  for (int i = 0; i < 2; ++i) {
    int unit = wid * 2 + i, sg = unit >> 2, dt = unit & 3;
    bf16x8 o = ldT(img + (sg * 32 + 8 * g + q) * 128 + ((dt ^ swz) << 5) + 8 * p, 512);
    int d = d0 + dt * 16 + (lane & 15), sc = (s0 >> 3) + sg * 4 + g;
    *(bf16x8*)(ws + WS_U + (d >> 2) * 16384 + sc * 64 + (d & 3) * 16) = o;
  }
#endif
}

__global__ __launch_bounds__(512, 2) void k_main(const u16* x, const u16* hk, const u16* Dv, const u16* w1, const u16* w2, char* ws, u16* out) {
#if defined(__HIP_DEVICE_COMPILE__)
  extern __shared__ __attribute__((aligned(16))) char smem_[];
  LAS char* smem = (LAS char*)smem_;
  const unsigned lds0 = (unsigned)(unsigned long long)smem;
  const int wid = __builtin_amdgcn_readfirstlane(threadIdx.x >> 6);
  const int b = blockIdx.x, xcd = b & 7, r = b >> 3;
  constexpr int GN = 32 / GM, XM = 16 / GM;
  const int li = r % GM, lj = r / GM;
  const int m = (xcd % XM) * GM + li, j = (xcd / XM) * GN + lj;
  const int kh = j >> 3, n2 = j & 7;
  GAS char* wsg = (GAS char*)ws;
  TS(0);
  if (wid >= 4) {
    const int lw = wid - 4, ph = (li + lj) & 3;
    const int lane = lane_id(), g = lane >> 4, q = (lane & 15) >> 2, p = lane & 3;
    const u32x4 rxn = mksrd(ws + WS_XN, S * D * 2), rw1 = mksrd(w1, D * F * 2);
    unsigned vx[4], vf[2], vh;
#pragma unroll
    for (int i = 0; i < 4; ++i) { int row = 8 * (4 * lw + i) + (lane >> 3); vx[i] = (m * 128 + row) * 2048 + (((lane & 7) ^ ((row >> 1) & 7)) << 4); }
    const int cbF = j * 448 + ((j & 1) ? 64 : 0), cbH = j * 448 + ((j & 1) ? 0 : 384);
#pragma unroll
    for (int i = 0; i < 2; ++i) { int row = 8 * (2 * lw + i) + (lane >> 3), swz = ((row >> 1) & 1) | (((row >> 3) & 1) << 1); vf[i] = row * 7168 + cbF + (((lane & 7) ^ (swz << 1)) << 4); }
    { int row = 16 * lw + (lane >> 2); vh = row * 7168 + cbH + (((lane & 3) ^ (((row >> 3) & 1) << 1)) << 4); }
    auto issue1 = [&](int t, int st) {
      const int ks = kperm(t, ph, 16), sx = ks * 128, sw = ks * 64 * 7168;
#pragma unroll
      for (int i = 0; i < 4; ++i) dma(lds0 + XO + st * 16384 + (4 * lw + i) * 1024, vx[i], rxn, sx);
#pragma unroll
      for (int l = 0; l < 3; ++l)
#pragma unroll
        for (int i = 0; i < 2; ++i) dma(lds0 + WFO + l * 24576 + st * 8192 + (2 * lw + i) * 1024, vf[i], rw1, sw + l * 128);
      dma(lds0 + WHO + st * 4096 + lw * 1024, vh, rw1, sw);
    };
    const int ch = 4 * b + lw;
    const unsigned vl = lane * 16;
    issue1(0, 0); issue1(1, 1);
    dma4(lds0 + SPO + lw * HIMG + 128, vl, mksrd(hk, S * D * 2), ch * 4096);
    issue1(2, 2);
    if (lane < 8) *(LAS u32x4*)(smem + SPO + lw * HIMG + lane * 16) = (u32x4){0, 0, 0, 0};
    const float dch = bflo(Dv[ch]);
    WAITV(26); BAR();
    WAITV(11); BAR();
    bf16x8 ca[16];
    {
      LAS char* pa = smem + SPO + lw * HIMG + 128 + 2 * (-48 + 32 * g + 4 * q + 16 * p);
#pragma unroll
      for (int t = 0; t < 16; ++t) ca[t] = ldT(pa + 256 * t, 32);
    }
    issue1(3, 0); WAITL0(); WAITV(11); BAR();
    dma4(lds0 + SPO + UPAD + lw * 4096, vl, mksrd(ws + WS_U, S * D * 2), b * 16384 + lw * 4096);
    if (lane < 10) *(LAS u32x4*)(smem + SPO + lw * 160 + lane * 16) = (u32x4){0, 0, 0, 0};
    int st = 1;
    for (int T = 2; T < 14; ++T) { issue1(T + 2, st); st = (st == 2) ? 0 : st + 1; WAITL0(); WAITV(11); BAR(); }
    WAITV(0); BAR();
    bf16x8 cb[32];
    {
      const int n0 = 128 + 48 - 32 * g - 4 * q + 4 * p - 16;
      LAS char* pb = smem + SPO + UPAD - 1024 + (n0 >> 3) * 64 + lw * 16 + (n0 & 7) * 2;
#pragma unroll
      for (int t = 0; t < 32; ++t) {
        s16x4 lo = __builtin_amdgcn_ds_read_tr16_b64_v4i16((LAS s16x4*)(pb + 512 * t + 128));
        s16x4 hi = __builtin_amdgcn_ds_read_tr16_b64_v4i16((LAS s16x4*)(pb + 512 * t));
        cb[t] = __builtin_bit_cast(bf16x8, (s16x8)__builtin_shufflevector(lo, hi, 0, 1, 2, 3, 4, 5, 6, 7));
      }
    }
    const u32x4 rw2 = mksrd(w2, D * F * 2), rh = mksrd(ws + WS_H, S * F * 2);
    unsigned vb[2][2];
#pragma unroll
    for (int im = 0; im < 2; ++im)
#pragma unroll
      for (int i = 0; i < 2; ++i) { int row = 16 * lw + 8 * i + (lane >> 3); vb[im][i] = (kh * 1792 + row) * 2048 + n2 * 256 + (im ^ kh) * 128 + (((lane & 7) ^ (((row >> 1) & 3) << 1)) << 4); }
    auto issueB = [&](int t, int s4) {
      const int ks = kperm(t, ph, 28);
#pragma unroll
      for (int im = 0; im < 2; ++im)
#pragma unroll
        for (int i = 0; i < 2; ++i) dma(lds0 + B2O + s4 * 16384 + im * 8192 + (2 * lw + i) * 1024, vb[im][i], rw2, ks * 64 * 2048);
    };
    auto issueA = [&](int t, int s4) {
      const int ks = kperm(t, ph, 28);
      dma4_sc1(lds0 + A2O + s4 * 16384 + lw * 4096, vl, rh, (m * 112 + kh * 56 + 2 * ks) * 8192 + lw * 4096);
    };
    WAITL0(); BAR();
    TS(1);
    issueB(0, 0); issueB(1, 1); issueB(2, 2); issueB(3, 3);
    {
      LAS char* py = smem + SPO + UPAD + (lane >> 3) * 64 + lw * 16 + (lane & 7) * 2;
      f32x4 prev = {0, 0, 0, 0};
      auto fin = [&](const f32x4& a, const f32x4& pv, int T) {
        float w1_ = lane >= 63 ? pv[1] : a[1], w2_ = lane >= 62 ? pv[2] : a[2], w3_ = lane >= 61 ? pv[3] : a[3];
        float z = w2_ + __int_as_float(__builtin_amdgcn_update_dpp(0, __float_as_int(w3_), 0x13C, 0xf, 0xf, false));
        z = w1_ + __int_as_float(__builtin_amdgcn_update_dpp(0, __float_as_int(z), 0x13C, 0xf, 0xf, false));
        z = a[0] + __int_as_float(__builtin_amdgcn_update_dpp(0, __float_as_int(z), 0x13C, 0xf, 0xf, false));
        const float u = bflo(*(LAS u16*)(py + 512 * T));
        *(LAS u16*)(py + 512 * T) = (u16)pk(z + u * dch, 0.f);
      };
#pragma unroll
      for (int P = 0; P < 16; ++P) {
        f32x4 a0 = {0, 0, 0, 0}, a1 = {0, 0, 0, 0};
#pragma unroll
        for (int t = 0; t <= P; ++t) { a0 = MFMA(ca[t], cb[2 * P - 2 * t], a0); a1 = MFMA(ca[t], cb[2 * P + 1 - 2 * t], a1); }
        fin(a0, prev, 2 * P); fin(a1, a0, 2 * P + 1); prev = a1;
      }
      const __amdgpu_buffer_rsrc_t ry = __builtin_amdgcn_make_buffer_rsrc((void*)(ws + WS_Y), 0, S * D * 2, 0x00020000);
#pragma unroll
      for (int i = 0; i < 4; ++i) {
        u32x4 o = *(LAS u32x4*)(smem + SPO + UPAD + (64 * i + lane) * 64 + lw * 16);
        __builtin_amdgcn_raw_buffer_store_b128(o, ry, vl + i * 1024, ch * 4096, 16);
      }
    }
    TS(2);
    WAITV(0);
    if (lane == 0) __hip_atomic_fetch_add((GAS unsigned*)(wsg + FL_Y + (b >> 4) * 256), 1u, RLX_AGENT);
    wait_ge((GAS unsigned*)(wsg + FL_H + (m * 2 + kh) * 256), 32);
    TS(3);
    issueA(0, 0); issueA(1, 1); issueA(2, 2); issueA(3, 3);
    WAITV(12); BAR();
    WAITV(8); BAR();
    issueA(4, 0); issueB(4, 0); WAITV(12); BAR();
    for (int T = 2; T < 25; ++T) { issueA(T + 3, (T + 3) & 3); issueB(T + 3, (T + 3) & 3); WAITV(16); BAR(); }
    wait_ge((GAS unsigned*)(wsg + FL_Y + (2 * n2 + kh) * 256), 64);
    {
      const u32x4 ry = mksrd(ws + WS_Y, S * D * 2);
#pragma unroll
      for (int i = 0; i < 4; ++i) {
        int row = 16 * lw + 4 * i + (lane >> 4), swz = (row & 3) | (((row >> 4) & 1) << 2);
        dma_sc1(lds0 + A2O + (4 * lw + i) * 1024, (128 * n2 + 64 * kh + row) * 4096 + m * 256 + (((lane & 15) ^ (swz << 1)) << 4), ry, 0);
      }
    }
    BAR();
    WAITV(0); BAR();
    TS(4); TSOUT(5);
  } else {
    __builtin_amdgcn_s_setprio(1);
    const int wr = wid >> 1, wc = wid & 1;
    {
      const int lane = lane_id(), fr = lane & 15, g = lane >> 4, q = (lane & 15) >> 2, p = lane & 3, swz = ((q >> 1) & 1) | ((g & 1) << 1);
      LAS char* xb[2][2]; LAS char* wb[4]; LAS char* hb[2];
#pragma unroll
      for (int ks = 0; ks < 2; ++ks)
#pragma unroll
        for (int h = 0; h < 2; ++h) { int mt = 4 * wr + ((2 * h) ^ (2 * wc)); xb[ks][h] = smem + XO + (16 * mt + fr) * 128 + (((4 * ks + g) ^ ((fr >> 1) & 7)) << 4); }
#pragma unroll
      for (int t = 0; t < 4; ++t) { int tt = 6 * wc + t; wb[t] = smem + WFO + (tt >> 2) * 24576 + (8 * g + q) * 128 + (((tt & 3) ^ swz) << 5) + 8 * p; }
#pragma unroll
      for (int t = 0; t < 2; ++t) hb[t] = smem + WHO + (8 * g + q) * 64 + ((t ^ (g & 1)) << 5) + 8 * p;
      f32x4 acc[6][4], acch[2][2];
#pragma unroll
      for (int n = 0; n < 6; ++n)
#pragma unroll
        for (int u = 0; u < 4; ++u) acc[n][u] = (f32x4){0, 0, 0, 0};
#pragma unroll
      for (int n = 0; n < 2; ++n)
#pragma unroll
        for (int u = 0; u < 2; ++u) acch[n][u] = (f32x4){0, 0, 0, 0};
      struct Fr { bf16x8 x[4]; bf16x8 w[8]; } P, Q;
      auto rd = [&](Fr& f, int st, int ks) {
#pragma unroll
        for (int u = 0; u < 4; ++u) f.x[u] = ldA(xb[ks][u >> 1] + st * 16384 + (u & 1) * 2048);
#pragma unroll
        for (int t = 0; t < 6; ++t) f.w[t] = ldT(wb[t & 3] + (t >> 2) * 24576 + st * 8192 + ks * 4096, 512);
#pragma unroll
        for (int t = 0; t < 2; ++t) f.w[6 + t] = ldT(hb[t] + st * 4096 + ks * 2048, 256);
      };
      auto mm = [&](Fr& f) {
#pragma unroll
        for (int n = 0; n < 6; ++n)
#pragma unroll
          for (int u = 0; u < 4; ++u) acc[n][u] = MFMA(f.w[n], f.x[u], acc[n][u]);
#pragma unroll
        for (int n = 0; n < 2; ++n)
#pragma unroll
          for (int u = 0; u < 2; ++u) acch[n][u] = MFMA(f.w[6 + n], f.x[u], acch[n][u]);
      };
      auto ilv = [&]() {
#pragma unroll
        for (int i = 0; i < 20; ++i) { __builtin_amdgcn_sched_group_barrier(0x8, 1, 0); __builtin_amdgcn_sched_group_barrier(0x100, 1, 0); }
        __builtin_amdgcn_sched_group_barrier(0x8, 8, 0);
      };
#define STEP1(st, nst) do { rd(Q, st, 1); mm(P); ilv(); SCHED(); WAITL0(); BAR(); SCHED(); rd(P, nst, 0); mm(Q); ilv(); SCHED(); } while (0)
      BAR();
      TS(1);
      rd(P, 0, 0); SCHED();
      for (int it = 0; it < 5; ++it) { STEP1(0, 1); STEP1(1, 2); STEP1(2, 0); }
      rd(Q, 0, 1); mm(P); ilv(); SCHED(); WAITL0(); BAR(); SCHED();
      mm(Q); SCHED();
      TS(2);
      const __amdgpu_buffer_rsrc_t rh = __builtin_amdgcn_make_buffer_rsrc((void*)(ws + WS_H), 0, S * F * 2, 0x00020000);
      const int vl = lane_id() * 16;
      const int kbF = m * 112 + 7 * j + (j & 1) + 3 * wc, kbH = m * 112 + 7 * j + ((j & 1) ? 0 : 6);
      auto put = [&](const f32x4& a, const f32x4& c, int kb, int u) {
        u32x4 o = {pk(silu(a[0]), silu(a[1])), pk(silu(a[2]), silu(a[3])), pk(silu(c[0]), silu(c[1])), pk(silu(c[2]), silu(c[3]))};
        __builtin_amdgcn_raw_buffer_store_b128(o, rh, vl, (kb * 8 + 4 * wr + (u ^ (2 * wc))) * 1024, 16);
      };
#pragma unroll
      for (int P2 = 0; P2 < 3; ++P2)
#pragma unroll
        for (int u = 0; u < 4; ++u) put(acc[2 * P2][u], acc[2 * P2 + 1][u], kbF + P2, u);
#pragma unroll
      for (int u = 0; u < 2; ++u) put(acch[0][u], acch[1][u], kbH, u);
      TS(3);
      WAITV(0);
      if (vl == 0) __hip_atomic_fetch_add((GAS unsigned*)(wsg + FL_H + (m * 2 + kh) * 256), 1u, RLX_AGENT);
      TS(4);
    }
    const int lane = lane_id(), fr = lane & 15, g = lane >> 4, q = (lane & 15) >> 2, p = lane & 3, swz = ((q >> 1) & 1) | ((g & 1) << 1);
    const int col0 = 128 * n2 + 64 * kh + 32 * wc + 16 * (g & 1) + 8 * (g >> 1);
    u32x4 xv[4], nv[4];
#pragma unroll
    for (int u = 0; u < 4; ++u) {
      const int o = (m * 128 + 64 * wr + 16 * u + fr) * D + col0;
      xv[u] = *(const u32x4*)(x + o); nv[u] = *(const u32x4*)(ws + WS_XN + 2 * o);
    }
    f32x4 acc[4][4];
#pragma unroll
    for (int n = 0; n < 4; ++n)
#pragma unroll
      for (int u = 0; u < 4; ++u) acc[n][u] = (f32x4){0, 0, 0, 0};
    LAS char* ab = smem + A2O + wr * 4096 + lane * 16;
    LAS char* bb[2];
#pragma unroll
    for (int t = 0; t < 2; ++t) bb[t] = smem + B2O + (4 * g + q) * 128 + (((2 * wc + t) ^ swz) << 5) + 8 * p;
    struct F2 { bf16x8 a[4]; bf16x8 w[4]; } P, Q;
    auto rd = [&](F2& f, int st, int kb) {
#pragma unroll
      for (int u = 0; u < 4; ++u) f.a[u] = ldA(ab + st * 16384 + kb * 8192 + u * 1024);
#pragma unroll
      for (int n = 0; n < 4; ++n) f.w[n] = ldT(bb[n & 1] + st * 16384 + (n >> 1) * 8192 + kb * 4096, 2048);
    };
    auto mm = [&](F2& f) {
#pragma unroll
      for (int n = 0; n < 4; ++n)
#pragma unroll
        for (int u = 0; u < 4; ++u) acc[n][u] = MFMA(f.w[n], f.a[u], acc[n][u]);
    };
    auto ilv = [&]() {
#pragma unroll
      for (int i = 0; i < 12; ++i) { __builtin_amdgcn_sched_group_barrier(0x8, 1, 0); __builtin_amdgcn_sched_group_barrier(0x100, 1, 0); }
      __builtin_amdgcn_sched_group_barrier(0x8, 4, 0);
    };
#define STEP2(st, nst) do { rd(Q, st, 1); mm(P); ilv(); SCHED(); WAITL0(); BAR(); SCHED(); rd(P, nst, 0); mm(Q); ilv(); SCHED(); } while (0)
    asm volatile("" : "+v"(xv[0]), "+v"(xv[1]), "+v"(xv[2]), "+v"(xv[3]), "+v"(nv[0]), "+v"(nv[1]), "+v"(nv[2]), "+v"(nv[3]));
    BAR();
    TS(5);
    rd(P, 0, 0); SCHED();
    for (int it = 0; it < 6; ++it) { STEP2(0, 1); STEP2(1, 2); STEP2(2, 3); STEP2(3, 0); }
    STEP2(0, 1); STEP2(1, 2); STEP2(2, 3);
    rd(Q, 3, 1); mm(P); ilv(); SCHED(); mm(Q); SCHED();
    TS(6);
    const __amdgpu_buffer_rsrc_t rp = __builtin_amdgcn_make_buffer_rsrc((void*)(ws + WS_P), 0, S * D * 4, 0x00020000);
    const int me = (m * 16 + j) * 4 + wid, pa = (m * 16 + (j ^ 8)) * 4 + wid;
#pragma unroll
    for (int n = 0; n < 2; ++n)
#pragma unroll
      for (int u = 0; u < 4; ++u) __builtin_amdgcn_raw_buffer_store_b128(__builtin_bit_cast(u32x4, acc[2 + n][u]), rp, lane * 16 + (n * 4 + u) * 1024, me * 8192, 16);
    bf16x8 yv[4];
    {
      const int row = 32 * wc + 16 * (g & 1) + 8 * (g >> 1) + q, sw = (row & 3) | (((row >> 4) & 1) << 2);
#pragma unroll
      for (int u = 0; u < 4; ++u) yv[u] = ldT(smem + A2O + row * 256 + (((4 * wr + u) ^ sw) << 5) + 8 * p, 1024);
    }
    WAITV(0);
    if (lane == 0) __hip_atomic_store((GAS unsigned*)(wsg + FL_P + me * 32), 1u, RLX_AGENT);
    TS(7);
    wait_ge((GAS unsigned*)(wsg + FL_P + pa * 32), 1);
    TS(8);
    u32x4 pp[2][4];
#pragma unroll
    for (int n = 0; n < 2; ++n)
#pragma unroll
      for (int u = 0; u < 4; ++u) pp[n][u] = __builtin_amdgcn_raw_buffer_load_b128(rp, lane * 16 + (n * 4 + u) * 1024, pa * 8192, 16);
#pragma unroll
    for (int u = 0; u < 4; ++u) {
      f32x4 a = acc[0][u] + __builtin_bit_cast(f32x4, pp[0][u]), c = acc[1][u] + __builtin_bit_cast(f32x4, pp[1][u]);
#pragma unroll
      for (int e = 0; e < 4; ++e) {
        u32x2 sw2 = __builtin_amdgcn_permlane16_swap(__float_as_uint(a[e]), __float_as_uint(c[e]), false, false);
        a[e] = __uint_as_float(sw2[0]); c[e] = __uint_as_float(sw2[1]);
      }
      const u32x4 y4 = __builtin_bit_cast(u32x4, yv[u]);
      u32x4 o;
      o[0] = pk(bflo(xv[u][0]) + bflo(nv[u][0]) * bflo(y4[0]) + a[0], bfhi(xv[u][0]) + bfhi(nv[u][0]) * bfhi(y4[0]) + a[1]);
      o[1] = pk(bflo(xv[u][1]) + bflo(nv[u][1]) * bflo(y4[1]) + a[2], bfhi(xv[u][1]) + bfhi(nv[u][1]) * bfhi(y4[1]) + a[3]);
      o[2] = pk(bflo(xv[u][2]) + bflo(nv[u][2]) * bflo(y4[2]) + c[0], bfhi(xv[u][2]) + bfhi(nv[u][2]) * bfhi(y4[2]) + c[1]);
      o[3] = pk(bflo(xv[u][3]) + bflo(nv[u][3]) * bflo(y4[3]) + c[2], bfhi(xv[u][3]) + bfhi(nv[u][3]) * bfhi(y4[3]) + c[3]);
      *(u32x4*)(out + (m * 128 + 64 * wr + 16 * u + fr) * D + col0) = o;
    }
    TS(9);
  }
#endif
}

extern "C" void kernel_launch(void* const* d_in, const int* in_sizes, int n_in, void* d_out, int out_size, void* d_ws, size_t ws_size, hipStream_t stream) {
  const u16* x = (const u16*)d_in[0];
  (void)hipFuncSetAttribute((const void*)k_main, hipFuncAttributeMaxDynamicSharedMemorySize, LDS_BYTES);
  k_prep<<<256, 512, 0, stream>>>(x, (const u16*)d_in[1], (const u16*)d_in[2], (const u16*)d_in[3], (char*)d_ws + (128u<<20));
  k_main<<<256, 512, LDS_BYTES, stream>>>(x, (const u16*)d_in[4], (const u16*)d_in[5], (const u16*)d_in[7], (const u16*)d_in[8], (char*)d_ws + (128u<<20), (u16*)d_out);
}
```

```cpp
#include <hip/hip_runtime.h>

__global__ __launch_bounds__(512, 2) void k_fused(const void* x, const void* k, const void* v, const void* sk, const void* hk, const void* dv,
                                                  const void* w1, const void* w2, void* ws, void* out) {
#if defined(__HIP_DEVICE_COMPILE__)
  asm volatile(
    "Lstart_%=:\n"
    "s_getpc_b64 s[54:55]\n"
    "s_bfe_u32 s51, s2, 196611\n"
    "v_lshl_add_u32 v1, v0, 3, s51\n"
    "v_lshlrev_b32 v1, 7, v1\n"
    "v_min_u32 v1, Lend_%=-Lstart_%=-24, v1\n"
    "global_load_dword v228, v1, s[54:55]\n"
    "s_load_dwordx16 s[4:19], s[0:1], 0\n"
    "s_load_dwordx4 s[20:23], s[0:1], 64\n"
    "v_readfirstlane_b32 s50, v0\n"
    "v_and_b32 v0, 63, v0\n"
    "s_lshr_b32 s51, s2, 3\n"
    "s_and_b32 s43, s51, 7\n"
    "s_lshr_b32 s44, s51, 3\n"
    "s_and_b32 s41, s2, 1\n"
    "s_lshl_b32 s41, s41, 3\n"
    "s_add_u32 s41, s41, s43\n"
    "s_bfe_u32 s42, s2, 131073\n"
    "s_lshl_b32 s42, s42, 2\n"
    "s_add_u32 s42, s42, s44\n"
    "s_bfe_u32 s40, s50, 131078\n"
    "s_add_u32 s63, s43, s44\n"
    "s_mov_b32 s47, 0x80000\n"
    "s_lshr_b32 s45, s42, 3\n"
    "s_and_b32 s46, s42, 7\n"
    "s_lshr_b32 s48, s40, 1\n"
    "s_and_b32 s49, s40, 1\n"
    "s_cmp_lt_u32 s50, 256\n"
    "s_cbranch_scc1 LG0_%=\n"
    "v_mov_b32 v255, v0\n"
    "v_lshrrev_b32 v1, 3, v0\n"
    "v_lshrrev_b32 v7, 4, v0\n"
    "v_and_b32 v3, 7, v0\n"
    "v_xor_b32 v4, v3, v7\n"
    "v_lshlrev_b32 v4, 4, v4\n"
    "v_lshl_add_u32 v4, v1, 11, v4\n"
    "s_lshl2_add_u32 s50, s41, s40\n"
    "s_lshl_b32 s50, s50, 16\n"
    "v_add_u32 v216, s50, v4\n"
    "v_xor_b32 v217, 64, v216\n"
    "v_add_u32 v218, 30720, v216\n"
    "v_add_u32 v219, 46080, v217\n"
    "v_add_u32 v217, 15360, v217\n"
    "s_mul_i32 s50, s42, 448\n"
    "s_mul_i32 s51, s40, 114688\n"
    "s_add_u32 s50, s50, s51\n"
    "s_and_b32 s51, s42, 1\n"
    "s_lshl_b32 s52, s51, 6\n"
    "s_add_u32 s52, s52, s50\n"
    "s_xor_b32 s51, s51, 1\n"
    "s_mul_i32 s51, s51, 384\n"
    "s_add_u32 s51, s51, s50\n"
    "v_and_b32 v4, 1, v7\n"
    "v_lshlrev_b32 v4, 1, v4\n"
    "v_xor_b32 v4, v4, v3\n"
    "v_lshlrev_b32 v4, 4, v4\n"
    "v_mul_u32_u24 v5, 7168, v1\n"
    "v_add3_u32 v224, v5, v4, s52\n"
    "v_xor_b32 v225, 64, v224\n"
    "v_add_u32 v225, 56320, v225\n"
    "v_lshrrev_b32 v4, 5, v0\n"
    "v_lshlrev_b32 v4, 1, v4\n"
    "v_and_b32 v9, 3, v0\n"
    "v_xor_b32 v4, v4, v9\n"
    "v_lshlrev_b32 v4, 4, v4\n"
    "v_lshrrev_b32 v5, 2, v0\n"
    "v_mul_u32_u24 v5, 7168, v5\n"
    "v_add3_u32 v226, v5, v4, s51\n"
    "s_lshl_b32 s60, s40, 12\n"
    "s_lshl_b32 s61, s40, 11\n"
    "v_mov_b32 v227, v0\n"
    "v_subrev_u32 v4, s63, v227\n"
    "v_and_b32 v4, 3, v4\n"
    "v_sub_u32 v5, v227, v4\n"
    "v_cmp_gt_u32 s[64:65], 15, v5\n"
    "v_cmp_eq_u32 vcc, 0, v4\n"
    "v_cmp_ge_u32 s[66:67], 1, v4\n"
    "s_nop 1\n"
    "v_cndmask_b32 v5, 0, -1, s[66:67]\n"
    "v_cndmask_b32 v5, v5, 1, vcc\n"
    "v_cndmask_b32 v5, 0, v5, s[64:65]\n"
    "v_add_u32 v227, v227, v5\n"
    "s_and_b32 s50, s41, 8\n"
    "v_xor_b32 v227, s50, v227\n"
    "s_add_u32 s61, s61, 49152\n"
    "v_readlane_b32 s50, v227, 4\n"
    "v_cmp_eq_u32 vcc, s41, v227\n"
    "s_lshl_b32 s62, s40, 10\n"
    "v_mov_b32 v4, s50\n"
    "v_cndmask_b32 v227, v227, v4, vcc\n"
    "v_writelane_b32 v227, s41, 4\n"
    "s_add_u32 s62, s62, 122880\n"
    "v_lshlrev_b32 v249, 4, v0\n"
    "s_lshl2_add_u32 s70, s2, s40\n"
    "s_waitcnt lgkmcnt(0)\n"
    "s_mov_b32 s28, s4\n"
    "s_and_b32 s29, s5, 0xffff\n"
    "s_mov_b32 s30, 4194304\n"
    "s_mov_b32 s31, 0x20000\n"
    "s_mov_b32 s32, s16\n"
    "s_and_b32 s33, s17, 0xffff\n"
    "s_mov_b32 s34, 7340032\n"
    "s_mov_b32 s35, 0x20000\n"
    "s_add_u32 m0, s60, 0\n"
    "v_readlane_b32 s50, v227, 0\n"
    "s_nop 0\n"
    "s_lshl_b32 s51, s50, 7\n"
    "s_mul_i32 s52, s50, 458752\n"
    "buffer_load_dwordx4 v216, s[28:31], s51 offen lds\n"
    "buffer_load_dwordx4 v217, s[28:31], s51 offen offset:1024 lds\n"
    "buffer_load_dwordx4 v218, s[28:31], s51 offen offset:2048 lds\n"
    "buffer_load_dwordx4 v219, s[28:31], s51 offen offset:3072 lds\n"
    "s_add_u32 m0, s61, 0\n"
    "s_add_u32 s51, s52, 0\n"
    "buffer_load_dwordx4 v224, s[32:35], s51 offen lds\n"
    "buffer_load_dwordx4 v225, s[32:35], s51 offen offset:1024 lds\n"
    "s_add_u32 m0, s61, 24576\n"
    "s_add_u32 s51, s52, 128\n"
    "buffer_load_dwordx4 v224, s[32:35], s51 offen lds\n"
    "buffer_load_dwordx4 v225, s[32:35], s51 offen offset:1024 lds\n"
    "s_add_u32 m0, s61, 49152\n"
    "s_add_u32 s51, s52, 256\n"
    "buffer_load_dwordx4 v224, s[32:35], s51 offen lds\n"
    "buffer_load_dwordx4 v225, s[32:35], s51 offen offset:1024 lds\n"
    "s_add_u32 m0, s62, 0\n"
    "s_nop 0\n"
    "buffer_load_dwordx4 v226, s[32:35], s52 offen lds\n"
    "s_mov_b32 s24, s20\n"
    "s_and_b32 s25, s21, 0xffff\n"
    "s_mov_b32 s26, 27787264\n"
    "s_mov_b32 s27, 0x20000\n"
    "s_lshl_b32 s56, s40, 28\n"
    "v_mov_b32 v252, s56\n"
    "s_lshr_b32 s53, s2, 4\n"
    "s_lshl2_add_u32 s53, s53, s40\n"
    "s_lshl_b32 s54, s53, 16\n"
    "s_and_b32 s55, s2, 15\n"
    "s_lshl_b32 s56, s55, 7\n"
    "s_add_u32 s54, s54, s56\n"
    "v_lshlrev_b32 v4, 4, v3\n"
    "v_lshl_add_u32 v4, v1, 13, v4\n"
    "v_add_u32 v239, s54, v4\n"
    "v_add_u32 v240, 4096, v239\n"
    "s_mov_b32 s36, s6\n"
    "s_and_b32 s37, s7, 0xffff\n"
    "s_mov_b32 s38, 4194304\n"
    "s_mov_b32 s39, 0x20000\n"
    "v_mov_b32 v250, 0\n"
    "v_bfe_u32 v8, v0, 2, 2\n"
    "v_lshlrev_b32 v245, 2, v3\n"
    "v_add_u32 v245, 155264, v245\n"
    "s_lshl2_add_u32 s54, s40, 155264\n"
    "v_mov_b32 v246, s54\n"
    "s_lshr_b32 s3, s2, 3\n"
    "s_lshl2_add_u32 s3, s3, s40\n"
    "s_and_b32 s54, s2, 7\n"
    "s_lshl3_add_u32 s3, s3, s54\n"
    "s_lshl_b32 s54, s3, 1\n"
    "s_and_b32 s54, s54, -4\n"
    "s_load_dword s71, s[14:15], s54\n"
    "s_and_b32 s54, s2, 1\n"
    "s_lshl_b32 s54, s54, 9\n"
    "s_add_u32 s54, s54, 155392\n"
    "v_lshl_add_u32 v241, v3, 6, s54\n"
    "v_bfe_u32 v4, v0, 2, 1\n"
    "v_bfe_u32 v5, v0, 3, 1\n"
    "v_lshl_add_u32 v4, v4, 1, v5\n"
    "v_lshlrev_b32 v4, 16, v4\n"
    "v_lshrrev_b32 v5, 4, v0\n"
    "v_lshl_add_u32 v4, v5, 6, v4\n"
    "v_and_b32 v5, 3, v0\n"
    "v_lshl_add_u32 v4, v5, 4, v4\n"
    "s_lshl_b32 s55, s55, 18\n"
    "s_lshl_b32 s53, s53, 8\n"
    "s_add_u32 s53, s53, s55\n"
    "s_add_u32 s53, s53, 524288\n"
    "v_add_u32 v242, s53, v4\n"
    "v_lshl_add_u32 v1, v7, 3, v8\n"
    "v_lshrrev_b32 v2, 1, v8\n"
    "v_and_b32 v5, 1, v7\n"
    "v_lshl_add_u32 v2, v5, 1, v2\n"
    "v_lshlrev_b32 v3, 3, v9\n"
    "s_mul_i32 s53, s40, 3\n"
    "s_add_u32 s54, s53, 0\n"
    "s_lshr_b32 s55, s54, 2\n"
    "s_mul_i32 s55, s55, 24576\n"
    "s_add_u32 s55, s55, 57344\n"
    "s_and_b32 s54, s54, 3\n"
    "v_xor_b32 v6, s54, v2\n"
    "v_lshl_add_u32 v6, v6, 5, v3\n"
    "v_lshl_add_u32 v6, v1, 7, v6\n"
    "v_add_u32 v192, s55, v6\n"
    "s_add_u32 s54, s53, 1\n"
    "s_lshr_b32 s55, s54, 2\n"
    "s_mul_i32 s55, s55, 24576\n"
    "s_add_u32 s55, s55, 57344\n"
    "s_and_b32 s54, s54, 3\n"
    "v_xor_b32 v6, s54, v2\n"
    "v_lshl_add_u32 v6, v6, 5, v3\n"
    "v_lshl_add_u32 v6, v1, 7, v6\n"
    "v_add_u32 v193, s55, v6\n"
    "s_add_u32 s54, s53, 2\n"
    "s_lshr_b32 s55, s54, 2\n"
    "s_mul_i32 s55, s55, 24576\n"
    "s_add_u32 s55, s55, 57344\n"
    "s_and_b32 s54, s54, 3\n"
    "v_xor_b32 v6, s54, v2\n"
    "v_lshl_add_u32 v6, v6, 5, v3\n"
    "v_lshl_add_u32 v6, v1, 7, v6\n"
    "v_add_u32 v194, s55, v6\n"
    "v_xor_b32 v6, s49, v5\n"
    "v_lshl_add_u32 v6, v6, 5, v3\n"
    "v_lshl_add_u32 v6, v1, 6, v6\n"
    "v_add_u32 v195, 126976, v6\n"
    "v_mov_b32 v196, 0x3f803f80\n"
    "v_mov_b32 v197, 0x3f803f80\n"
    "v_mov_b32 v198, 0x3f803f80\n"
    "v_mov_b32 v199, 0x3f803f80\n"
    "s_lshl4_add_u32 s54, s42, s41\n"
    "s_mul_i32 s54, s54, 5\n"
    "s_add_u32 s54, s54, s40\n"
    "s_lshl_b32 s54, s54, 8\n"
    "s_add_u32 s54, s54, 196608\n"
    "v_lshl_add_u32 v4, v7, 2, v9\n"
    "v_lshl_add_u32 v238, v4, 4, s54\n"
    "v_and_b32 v4, 15, v0\n"
    "v_mul_u32_u24 v6, 5120, v7\n"
    "v_lshl_add_u32 v6, v4, 4, v6\n"
    "s_mul_i32 s53, s42, 20480\n"
    "s_lshl_b32 s54, s40, 8\n"
    "s_add_u32 s53, s53, s54\n"
    "s_add_u32 s53, s53, 196608\n"
    "v_add_u32 v243, s53, v6\n"
    "s_add_u32 s54, s54, 154240\n"
    "v_add_u32 v244, s54, v249\n"
    "s_lshl_b32 s53, s48, 12\n"
    "v_add_u32 v230, s53, v249\n"
    "v_lshl_add_u32 v6, v7, 2, v8\n"
    "v_lshl_add_u32 v6, v6, 7, v3\n"
    "s_lshl_b32 s53, s49, 1\n"
    "v_xor_b32 v4, s53, v2\n"
    "v_lshl_add_u32 v6, v4, 5, v6\n"
    "v_add_u32 v231, 65536, v6\n"
    "v_xor_b32 v232, 32, v231\n"
    "v_sub_u32 v4, v9, v1\n"
    "v_add_u32 v4, 40, v4\n"
    "v_lshrrev_b32 v6, 1, v4\n"
    "v_and_b32 v4, 1, v4\n"
    "v_lshlrev_b32 v4, 3, v4\n"
    "v_lshl_add_u32 v4, v6, 6, v4\n"
    "s_lshl4_add_u32 s53, s40, 134784\n"
    "v_add_u32 v233, s53, v4\n"
    "v_lshrrev_b32 v4, 3, v0\n"
    "v_and_b32 v6, 7, v0\n"
    "v_lshlrev_b32 v253, 7, v0\n"
    "v_lshlrev_b32 v6, 1, v6\n"
    "v_lshl_add_u32 v4, v4, 6, v6\n"
    "s_add_u32 s53, s53, 1024\n"
    "v_add_u32 v229, s53, v4\n"
    "s_lshl_b32 s53, s40, 28\n"
    "s_add_u32 s53, s53, 256\n"
    "v_add_u32 v253, s53, v253\n"
    "s_mul_i32 s53, s46, 7\n"
    "v_lshrrev_b32 v234, 1, v255\n"
    "v_subrev_u32 v6, 7, v234\n"
    "v_subrev_u32 v4, s63, v6\n"
    "v_and_b32 v4, 3, v4\n"
    "v_sub_u32 v5, v6, v4\n"
    "v_cmp_gt_u32 s[64:65], 19, v5\n"
    "v_cmp_eq_u32 vcc, 0, v4\n"
    "v_cmp_ge_u32 s[66:67], 2, v4\n"
    "s_nop 1\n"
    "v_cndmask_b32 v5, 0, -1, s[66:67]\n"
    "v_cndmask_b32 v5, v5, 2, vcc\n"
    "v_cndmask_b32 v5, 0, v5, s[64:65]\n"
    "v_add_u32 v6, v6, v5\n"
    "v_add_u32 v6, 7, v6\n"
    "v_cmp_gt_u32 vcc, 7, v234\n"
    "s_nop 1\n"
    "v_cndmask_b32 v234, v6, v234, vcc\n"
    "v_and_b32 v4, 1, v255\n"
    "v_lshl_add_u32 v234, v234, 1, v4\n"
    "s_cmp_eq_u32 s46, 1\n"
    "s_cselect_b32 s64, 3129426704, 0\n"
    "s_cselect_b32 s65, 14456402, 0\n"
    "s_cselect_b32 s66, 0, 3129431073\n"
    "s_cselect_b32 s67, 0, 14454883\n"
    "s_or_b64 s[64:65], s[64:65], s[66:67]\n"
    "v_subrev_u32 v4, 7, v234\n"
    "v_lshlrev_b32 v5, 2, v4\n"
    "v_lshrrev_b64 v[220:221], v5, s[64:65]\n"
    "v_and_b32 v5, 15, v220\n"
    "v_add_u32 v5, 7, v5\n"
    "v_cmp_gt_u32 vcc, 14, v4\n"
    "s_nop 1\n"
    "v_cndmask_b32 v234, v234, v5, vcc\n"
    "v_add_u32 v4, s53, v234\n"
    "v_subrev_u32 v5, s53, v234\n"
    "v_cmp_gt_u32 vcc, 7, v5\n"
    "s_nop 1\n"
    "v_cndmask_b32 v5, v234, v5, vcc\n"
    "v_cmp_gt_u32 vcc, 7, v234\n"
    "s_nop 1\n"
    "v_cndmask_b32 v234, v5, v4, vcc\n"
    "v_lshrrev_b32 v4, 4, v255\n"
    "v_lshlrev_b32 v4, 1, v4\n"
    "v_and_b32 v5, 7, v255\n"
    "v_xor_b32 v4, v4, v5\n"
    "v_lshlrev_b32 v4, 4, v4\n"
    "v_lshrrev_b32 v5, 3, v255\n"
    "v_lshl_add_u32 v4, v5, 11, v4\n"
    "s_mul_i32 s53, s45, 3670016\n"
    "s_lshl_b32 s54, s46, 8\n"
    "s_add_u32 s53, s53, s54\n"
    "v_add_u32 v220, s53, v4\n"
    "v_add_u32 v221, 15360, v220\n"
    "v_add_u32 v222, 30720, v220\n"
    "v_add_u32 v223, 46080, v220\n"
    "s_lshl2_add_u32 s53, s41, s44\n"
    "s_lshl2_add_u32 s53, s53, s40\n"
    "s_mul_i32 s53, s53, 14336\n"
    "s_lshr_b32 s54, s46, 2\n"
    "s_lshl_b32 s54, s54, 10\n"
    "s_add_u32 s53, s53, s54\n"
    "s_mul_i32 s54, s45, 3670016\n"
    "s_add_u32 s44, s53, s54\n"
    "s_lshl_b32 s63, s40, 12\n"
    "s_add_u32 s43, s63, 57344\n"
    "s_cmp_eq_u32 s40, 2\n"
    "s_cselect_b32 s43, 155648, s43\n"
    "s_cmp_eq_u32 s40, 3\n"
    "s_cselect_b32 s43, 159744, s43\n"
    "s_add_u32 s63, s63, 135808\n"
    "v_lshlrev_b32 v1, 3, v1\n"
    "v_lshl_add_u32 v1, v9, 5, v1\n"
    "s_sub_u32 s53, s43, 96\n"
    "v_add_u32 v235, s53, v1\n"
    "v_mov_b32 v5, 135168\n"
    "v_cmp_gt_u32 vcc, 96, v1\n"
    "s_nop 1\n"
    "v_cndmask_b32 v236, v235, v5, vcc\n"
    "v_cmp_gt_u32 vcc, 64, v1\n"
    "s_nop 1\n"
    "v_cndmask_b32 v237, v235, v5, vcc\n"
    "s_mov_b32 s64, 0\n"
    "s_mov_b32 s65, 0x80000000\n"
    "s_mov_b32 s66, 0\n"
    "s_mov_b32 s67, 0xc0000000\n"
    "s_mov_b32 s68, 0\n"
    "s_mov_b32 s69, 0xe0000000\n"
    "s_waitcnt lgkmcnt(0)\n"
    "s_and_b32 s53, s3, 1\n"
    "s_lshl_b32 s53, s53, 4\n"
    "s_lshr_b32 s71, s71, s53\n"
    "s_lshl_b32 s71, s71, 16\n"
    "s_add_u32 m0, s60, 16384\n"
    "v_readlane_b32 s50, v227, 1\n"
    "s_nop 0\n"
    "s_lshl_b32 s51, s50, 7\n"
    "s_mul_i32 s52, s50, 458752\n"
    "buffer_load_dwordx4 v216, s[28:31], s51 offen lds\n"
    "buffer_load_dwordx4 v217, s[28:31], s51 offen offset:1024 lds\n"
    "buffer_load_dwordx4 v218, s[28:31], s51 offen offset:2048 lds\n"
    "buffer_load_dwordx4 v219, s[28:31], s51 offen offset:3072 lds\n"
    "s_add_u32 m0, s61, 8192\n"
    "s_add_u32 s51, s52, 0\n"
    "buffer_load_dwordx4 v224, s[32:35], s51 offen lds\n"
    "buffer_load_dwordx4 v225, s[32:35], s51 offen offset:1024 lds\n"
    "s_add_u32 m0, s61, 32768\n"
    "s_add_u32 s51, s52, 128\n"
    "buffer_load_dwordx4 v224, s[32:35], s51 offen lds\n"
    "buffer_load_dwordx4 v225, s[32:35], s51 offen offset:1024 lds\n"
    "buffer_load_dword v251, v252, s[24:27], 0 offen sc1\n"
    "s_add_u32 s51, s52, 256\n"
    "s_add_u32 m0, s61, 57344\n"
    "s_waitcnt vmcnt(9)\n"
    "s_waitcnt lgkmcnt(0)\n"
    "s_barrier\n"
    "buffer_load_dwordx4 v224, s[32:35], s51 offen lds\n"
    "buffer_load_dwordx4 v225, s[32:35], s51 offen offset:1024 lds\n"
    "s_add_u32 m0, s62, 4096\n"
    "s_nop 0\n"
    "buffer_load_dwordx4 v226, s[32:35], s52 offen lds\n"
    "s_add_u32 m0, s60, 32768\n"
    "v_readlane_b32 s50, v227, 2\n"
    "s_nop 0\n"
    "s_lshl_b32 s51, s50, 7\n"
    "s_mul_i32 s52, s50, 458752\n"
    "buffer_load_dwordx4 v216, s[28:31], s51 offen lds\n"
    "buffer_load_dwordx4 v217, s[28:31], s51 offen offset:1024 lds\n"
    "buffer_load_dwordx4 v218, s[28:31], s51 offen offset:2048 lds\n"
    "buffer_load_dwordx4 v219, s[28:31], s51 offen offset:3072 lds\n"
    "s_add_u32 m0, s61, 16384\n"
    "s_add_u32 s51, s52, 0\n"
    "buffer_load_dwordx4 v224, s[32:35], s51 offen lds\n"
    "buffer_load_dwordx4 v225, s[32:35], s51 offen offset:1024 lds\n"
    "s_add_u32 m0, s61, 40960\n"
    "s_add_u32 s51, s52, 128\n"
    "buffer_load_dwordx4 v224, s[32:35], s51 offen lds\n"
    "buffer_load_dwordx4 v225, s[32:35], s51 offen offset:1024 lds\n"
    "s_add_u32 m0, s61, 65536\n"
    "s_add_u32 s51, s52, 256\n"
    "buffer_load_dwordx4 v224, s[32:35], s51 offen lds\n"
    "buffer_load_dwordx4 v225, s[32:35], s51 offen offset:1024 lds\n"
    "s_add_u32 m0, s62, 8192\n"
    "s_nop 0\n"
    "buffer_load_dwordx4 v226, s[32:35], s52 offen lds\n"
    "v_readlane_b32 s50, v227, 3\n"
    "s_nop 0\n"
    "s_lshl_b32 s51, s50, 7\n"
    "s_mul_i32 s52, s50, 458752\n"
    "s_add_u32 m0, s60, 0\n"
    "s_waitcnt vmcnt(11)\n"
    "s_waitcnt lgkmcnt(0)\n"
    "s_barrier\n"
    "buffer_load_dwordx4 v216, s[28:31], s51 offen lds\n"
    "buffer_load_dwordx4 v217, s[28:31], s51 offen offset:1024 lds\n"
    "buffer_load_dwordx4 v218, s[28:31], s51 offen offset:2048 lds\n"
    "buffer_load_dwordx4 v219, s[28:31], s51 offen offset:3072 lds\n"
    "s_add_u32 m0, s61, 0\n"
    "s_add_u32 s51, s52, 0\n"
    "buffer_load_dwordx4 v224, s[32:35], s51 offen lds\n"
    "buffer_load_dwordx4 v225, s[32:35], s51 offen offset:1024 lds\n"
    "s_add_u32 m0, s61, 24576\n"
    "s_add_u32 s51, s52, 128\n"
    "buffer_load_dwordx4 v224, s[32:35], s51 offen lds\n"
    "buffer_load_dwordx4 v225, s[32:35], s51 offen offset:1024 lds\n"
    "s_add_u32 m0, s61, 49152\n"
    "s_add_u32 s51, s52, 256\n"
    "buffer_load_dwordx4 v224, s[32:35], s51 offen lds\n"
    "buffer_load_dwordx4 v225, s[32:35], s51 offen offset:1024 lds\n"
    "s_add_u32 m0, s62, 0\n"
    "s_nop 0\n"
    "buffer_load_dwordx4 v226, s[32:35], s52 offen lds\n"
    "buffer_load_dwordx4 v[76:79], v239, s[36:39], 0 offen offset:0\n"
    "buffer_load_dwordx4 v[80:83], v239, s[36:39], 0 offen offset:2048\n"
    "buffer_load_dwordx4 v[84:87], v240, s[36:39], 0 offen offset:0\n"
    "buffer_load_dwordx4 v[88:91], v240, s[36:39], 0 offen offset:2048\n"
    "v_subrev_u32 v247, 6144, v239\n"
    "buffer_load_dwordx4 v[64:67], v247, s[36:39], 0 offen\n"
    "v_subrev_u32 v247, 4096, v239\n"
    "buffer_load_dwordx4 v[68:71], v247, s[36:39], 0 offen\n"
    "v_subrev_u32 v247, 2048, v239\n"
    "buffer_load_dwordx4 v[72:75], v247, s[36:39], 0 offen\n"
    "s_mov_b32 m0, 155392\n"
    "s_bfe_u32 s58, s2, 196609\n"
    "s_lshl_b32 s58, s58, 10\n"
    "v_add_u32 v247, s58, v249\n"
    "global_load_lds_dwordx4 v247, s[10:11]\n"
    "v_readlane_b32 s50, v227, 4\n"
    "s_nop 0\n"
    "s_lshl_b32 s51, s50, 7\n"
    "s_mul_i32 s52, s50, 458752\n"
    "s_add_u32 m0, s60, 16384\n"
    "s_waitcnt vmcnt(19)\n"
    "s_waitcnt lgkmcnt(0)\n"
    "s_barrier\n"
    "buffer_load_dwordx4 v216, s[28:31], s51 offen lds\n"
    "buffer_load_dwordx4 v217, s[28:31], s51 offen offset:1024 lds\n"
    "buffer_load_dwordx4 v218, s[28:31], s51 offen offset:2048 lds\n"
    "buffer_load_dwordx4 v219, s[28:31], s51 offen offset:3072 lds\n"
    "s_add_u32 m0, s61, 8192\n"
    "s_add_u32 s51, s52, 0\n"
    "buffer_load_dwordx4 v224, s[32:35], s51 offen lds\n"
    "buffer_load_dwordx4 v225, s[32:35], s51 offen offset:1024 lds\n"
    "s_add_u32 m0, s61, 32768\n"
    "s_add_u32 s51, s52, 128\n"
    "buffer_load_dwordx4 v224, s[32:35], s51 offen lds\n"
    "buffer_load_dwordx4 v225, s[32:35], s51 offen offset:1024 lds\n"
    "s_add_u32 m0, s61, 57344\n"
    "s_add_u32 s51, s52, 256\n"
    "buffer_load_dwordx4 v224, s[32:35], s51 offen lds\n"
    "buffer_load_dwordx4 v225, s[32:35], s51 offen offset:1024 lds\n"
    "s_add_u32 m0, s62, 4096\n"
    "s_nop 0\n"
    "buffer_load_dwordx4 v226, s[32:35], s52 offen lds\n"
    "global_load_dwordx4 v[92:95], v239, s[8:9] offset:0 nt\n"
    "global_load_dwordx4 v[96:99], v239, s[8:9] offset:2048 nt\n"
    "global_load_dwordx4 v[100:103], v240, s[8:9] offset:0 nt\n"
    "global_load_dwordx4 v[104:107], v240, s[8:9] offset:2048 nt\n"
    "v_readlane_b32 s50, v227, 5\n"
    "s_nop 0\n"
    "s_lshl_b32 s51, s50, 7\n"
    "s_mul_i32 s52, s50, 458752\n"
    "s_add_u32 m0, s60, 32768\n"
    "s_waitcnt vmcnt(23)\n"
    "s_waitcnt lgkmcnt(0)\n"
    "s_barrier\n"
    "buffer_load_dwordx4 v216, s[28:31], s51 offen lds\n"
    "s_waitcnt vmcnt(16)\n"
    "ds_read_b128 v[108:111], v241 offset:0\n"
    "ds_read_b128 v[112:115], v241 offset:16\n"
    "ds_read_b128 v[116:119], v241 offset:32\n"
    "ds_read_b128 v[120:123], v241 offset:48\n"
    "v_mov_b32 v164, 0xffff0000\n"
    "buffer_load_dwordx4 v217, s[28:31], s51 offen offset:1024 lds\n"
    "v_lshlrev_b32 v148, 16, v64\n"
    "v_and_b32 v149, v164, v64\n"
    "v_lshlrev_b32 v150, 16, v68\n"
    "v_and_b32 v151, v164, v68\n"
    "v_lshlrev_b32 v152, 16, v72\n"
    "v_and_b32 v153, v164, v72\n"
    "buffer_load_dwordx4 v218, s[28:31], s51 offen offset:2048 lds\n"
    "v_lshlrev_b32 v154, 16, v76\n"
    "v_and_b32 v155, v164, v76\n"
    "v_lshlrev_b32 v156, 16, v80\n"
    "v_and_b32 v157, v164, v80\n"
    "v_lshlrev_b32 v158, 16, v84\n"
    "v_and_b32 v159, v164, v84\n"
    "buffer_load_dwordx4 v219, s[28:31], s51 offen offset:3072 lds\n"
    "v_lshlrev_b32 v160, 16, v88\n"
    "v_and_b32 v161, v164, v88\n"
    "s_waitcnt lgkmcnt(0)\n"
    "v_lshlrev_b32 v140, 16, v108\n"
    "v_and_b32 v141, v164, v108\n"
    "v_lshlrev_b32 v142, 16, v109\n"
    "s_add_u32 m0, s61, 16384\n"
    "s_add_u32 s51, s52, 0\n"
    "buffer_load_dwordx4 v224, s[32:35], s51 offen lds\n"
    "v_and_b32 v143, v164, v109\n"
    "v_lshlrev_b32 v144, 16, v110\n"
    "v_and_b32 v145, v164, v110\n"
    "v_lshlrev_b32 v146, 16, v111\n"
    "v_and_b32 v147, v164, v111\n"
    "v_mul_f32 v16, v148, v143\n"
    "buffer_load_dwordx4 v225, s[32:35], s51 offen offset:1024 lds\n"
    "v_mul_f32 v17, v149, v147\n"
    "v_mul_f32 v24, v150, v143\n"
    "v_mul_f32 v25, v151, v147\n"
    "v_mul_f32 v32, v152, v143\n"
    "v_mul_f32 v33, v153, v147\n"
    "v_mul_f32 v40, v154, v143\n"
    "s_add_u32 m0, s61, 40960\n"
    "s_add_u32 s51, s52, 128\n"
    "buffer_load_dwordx4 v224, s[32:35], s51 offen lds\n"
    "v_mul_f32 v41, v155, v147\n"
    "v_fmac_f32 v16, v150, v142\n"
    "v_fmac_f32 v17, v151, v146\n"
    "v_fmac_f32 v24, v152, v142\n"
    "v_fmac_f32 v25, v153, v146\n"
    "v_fmac_f32 v32, v154, v142\n"
    "buffer_load_dwordx4 v225, s[32:35], s51 offen offset:1024 lds\n"
    "v_fmac_f32 v33, v155, v146\n"
    "v_fmac_f32 v40, v156, v142\n"
    "v_fmac_f32 v41, v157, v146\n"
    "v_fmac_f32 v16, v152, v141\n"
    "v_fmac_f32 v17, v153, v145\n"
    "v_fmac_f32 v24, v154, v141\n"
    "s_add_u32 m0, s61, 65536\n"
    "s_add_u32 s51, s52, 256\n"
    "buffer_load_dwordx4 v224, s[32:35], s51 offen lds\n"
    "v_fmac_f32 v25, v155, v145\n"
    "v_fmac_f32 v32, v156, v141\n"
    "v_fmac_f32 v33, v157, v145\n"
    "v_fmac_f32 v40, v158, v141\n"
    "v_fmac_f32 v41, v159, v145\n"
    "v_fmac_f32 v16, v154, v140\n"
    "buffer_load_dwordx4 v225, s[32:35], s51 offen offset:1024 lds\n"
    "v_fmac_f32 v17, v155, v144\n"
    "v_fmac_f32 v24, v156, v140\n"
    "v_fmac_f32 v25, v157, v144\n"
    "v_fmac_f32 v32, v158, v140\n"
    "v_fmac_f32 v33, v159, v144\n"
    "v_fmac_f32 v40, v160, v140\n"
    "s_add_u32 m0, s62, 8192\n"
    "s_nop 0\n"
    "buffer_load_dwordx4 v226, s[32:35], s52 offen lds\n"
    "v_fmac_f32 v41, v161, v144\n"
    "v_lshlrev_b32 v148, 16, v65\n"
    "v_and_b32 v149, v164, v65\n"
    "v_lshlrev_b32 v150, 16, v69\n"
    "v_and_b32 v151, v164, v69\n"
    "v_lshlrev_b32 v152, 16, v73\n"
    "s_lshl_b32 s53, s3, 12\n"
    "v_lshl_add_u32 v247, v255, 7, s53\n"
    "s_and_b32 s53, s2, 1\n"
    "s_sub_u32 s53, s53, 1\n"
    "v_and_b32 v247, s53, v247\n"
    "global_load_dword v228, v247, s[12:13]\n"
    "s_cmp_lg_u32 s70, 0\n"
    "s_cselect_b32 s53, 0x10000000, 0\n"
    "v_mov_b32 v248, s53\n"
    "s_mov_b64 exec, 1\n"
    "v_mov_b32 v247, 1\n"
    "buffer_atomic_add v247, v248, s[24:27], 0 offen sc1\n"
    "s_mov_b64 exec, -1\n"
    "v_readlane_b32 s50, v227, 6\n"
    "s_nop 0\n"
    "s_lshl_b32 s51, s50, 7\n"
    "s_mul_i32 s52, s50, 458752\n"
    "s_add_u32 m0, s60, 0\n"
    "s_waitcnt vmcnt(17)\n"
    "s_waitcnt lgkmcnt(0)\n"
    "s_barrier\n"
    "buffer_load_dwordx4 v216, s[28:31], s51 offen lds\n"
    "s_setprio 1\n"
    "ds_read_b64_tr_b16 v[48:49], v192 offset:0\n"
    "ds_read_b64_tr_b16 v[50:51], v192 offset:512\n"
    "v_and_b32 v153, v164, v73\n"
    "v_lshlrev_b32 v154, 16, v77\n"
    "v_and_b32 v155, v164, v77\n"
    "v_lshlrev_b32 v156, 16, v81\n"
    "v_and_b32 v157, v164, v81\n"
    "v_lshlrev_b32 v158, 16, v85\n"
    "buffer_load_dwordx4 v217, s[28:31], s51 offen offset:1024 lds\n"
    "ds_read_b64_tr_b16 v[52:53], v193 offset:0\n"
    "ds_read_b64_tr_b16 v[54:55], v193 offset:512\n"
    "v_and_b32 v159, v164, v85\n"
    "v_lshlrev_b32 v160, 16, v89\n"
    "v_and_b32 v161, v164, v89\n"
    "v_lshlrev_b32 v140, 16, v112\n"
    "v_and_b32 v141, v164, v112\n"
    "v_lshlrev_b32 v142, 16, v113\n"
    "buffer_load_dwordx4 v218, s[28:31], s51 offen offset:2048 lds\n"
    "ds_read_b64_tr_b16 v[56:57], v194 offset:0\n"
    "ds_read_b64_tr_b16 v[58:59], v194 offset:512\n"
    "v_and_b32 v143, v164, v113\n"
    "v_lshlrev_b32 v144, 16, v114\n"
    "v_and_b32 v145, v164, v114\n"
    "v_lshlrev_b32 v146, 16, v115\n"
    "v_and_b32 v147, v164, v115\n"
    "v_mul_f32 v18, v148, v143\n"
    "buffer_load_dwordx4 v219, s[28:31], s51 offen offset:3072 lds\n"
    "ds_read_b64_tr_b16 v[60:61], v195 offset:0\n"
    "ds_read_b64_tr_b16 v[62:63], v195 offset:256\n"
    "v_mul_f32 v19, v149, v147\n"
    "v_mul_f32 v26, v150, v143\n"
    "v_mul_f32 v27, v151, v147\n"
    "v_mul_f32 v34, v152, v143\n"
    "v_mul_f32 v35, v153, v147\n"
    "v_mul_f32 v42, v154, v143\n"
    "s_add_u32 m0, s61, 0\n"
    "s_add_u32 s51, s52, 0\n"
    "buffer_load_dwordx4 v224, s[32:35], s51 offen lds\n"
    "ds_read_b64_tr_b16 v[124:125], v192 offset:4096\n"
    "ds_read_b64_tr_b16 v[126:127], v192 offset:4608\n"
    "v_mul_f32 v43, v155, v147\n"
    "v_fmac_f32 v18, v150, v142\n"
    "v_fmac_f32 v19, v151, v146\n"
    "v_fmac_f32 v26, v152, v142\n"
    "v_fmac_f32 v27, v153, v146\n"
    "v_fmac_f32 v34, v154, v142\n"
    "buffer_load_dwordx4 v225, s[32:35], s51 offen offset:1024 lds\n"
    "ds_read_b64_tr_b16 v[128:129], v193 offset:4096\n"
    "ds_read_b64_tr_b16 v[130:131], v193 offset:4608\n"
    "v_fmac_f32 v35, v155, v146\n"
    "v_fmac_f32 v42, v156, v142\n"
    "v_fmac_f32 v43, v157, v146\n"
    "v_fmac_f32 v18, v152, v141\n"
    "v_fmac_f32 v19, v153, v145\n"
    "v_fmac_f32 v26, v154, v141\n"
    "s_add_u32 m0, s61, 24576\n"
    "s_add_u32 s51, s52, 128\n"
    "buffer_load_dwordx4 v224, s[32:35], s51 offen lds\n"
    "ds_read_b64_tr_b16 v[132:133], v194 offset:4096\n"
    "ds_read_b64_tr_b16 v[134:135], v194 offset:4608\n"
    "v_fmac_f32 v27, v155, v145\n"
    "v_fmac_f32 v34, v156, v141\n"
    "v_fmac_f32 v35, v157, v145\n"
    "v_fmac_f32 v42, v158, v141\n"
    "v_fmac_f32 v43, v159, v145\n"
    "v_fmac_f32 v18, v154, v140\n"
    "buffer_load_dwordx4 v225, s[32:35], s51 offen offset:1024 lds\n"
    "ds_read_b64_tr_b16 v[136:137], v195 offset:2048\n"
    "ds_read_b64_tr_b16 v[138:139], v195 offset:2304\n"
    "s_setprio 0\n"
    "v_fmac_f32 v19, v155, v144\n"
    "v_fmac_f32 v26, v156, v140\n"
    "v_fmac_f32 v27, v157, v144\n"
    "v_fmac_f32 v34, v158, v140\n"
    "v_fmac_f32 v35, v159, v144\n"
    "v_fmac_f32 v42, v160, v140\n"
    "s_add_u32 m0, s61, 49152\n"
    "s_add_u32 s51, s52, 256\n"
    "buffer_load_dwordx4 v224, s[32:35], s51 offen lds\n"
    "v_fmac_f32 v43, v161, v144\n"
    "v_lshlrev_b32 v148, 16, v66\n"
    "v_and_b32 v149, v164, v66\n"
    "v_lshlrev_b32 v150, 16, v70\n"
    "v_and_b32 v151, v164, v70\n"
    "v_lshlrev_b32 v152, 16, v74\n"
    "buffer_load_dwordx4 v225, s[32:35], s51 offen offset:1024 lds\n"
    "v_and_b32 v153, v164, v74\n"
    "v_lshlrev_b32 v154, 16, v78\n"
    "v_and_b32 v155, v164, v78\n"
    "v_lshlrev_b32 v156, 16, v82\n"
    "v_and_b32 v157, v164, v82\n"
    "v_lshlrev_b32 v158, 16, v86\n"
    "s_add_u32 m0, s62, 0\n"
    "s_nop 0\n"
    "buffer_load_dwordx4 v226, s[32:35], s52 offen lds\n"
    "v_and_b32 v159, v164, v86\n"
    "v_lshlrev_b32 v160, 16, v90\n"
    "v_and_b32 v161, v164, v90\n"
    "v_lshlrev_b32 v140, 16, v116\n"
    "v_and_b32 v141, v164, v116\n"
    "v_lshlrev_b32 v142, 16, v117\n"
    "v_readlane_b32 s50, v227, 7\n"
    "s_nop 0\n"
    "s_lshl_b32 s51, s50, 7\n"
    "s_mul_i32 s52, s50, 458752\n"
    "s_add_u32 m0, s60, 16384\n"
    "s_waitcnt vmcnt(13)\n"
    "s_waitcnt lgkmcnt(0)\n"
    "s_barrier\n"
    "buffer_load_dwordx4 v216, s[28:31], s51 offen lds\n"
    "s_setprio 1\n"
    "v_mfma_f32_16x16x32_bf16 v[200:203], v[48:51], v[196:199], 0\n"
    "v_and_b32 v143, v164, v117\n"
    "v_lshlrev_b32 v144, 16, v118\n"
    "v_and_b32 v145, v164, v118\n"
    "v_lshlrev_b32 v146, 16, v119\n"
    "v_and_b32 v147, v164, v119\n"
    "v_mul_f32 v20, v148, v143\n"
    "buffer_load_dwordx4 v217, s[28:31], s51 offen offset:1024 lds\n"
    "v_mfma_f32_16x16x32_bf16 v[204:207], v[52:55], v[196:199], 0\n"
    "v_mul_f32 v21, v149, v147\n"
    "v_mul_f32 v28, v150, v143\n"
    "v_mul_f32 v29, v151, v147\n"
    "v_mul_f32 v36, v152, v143\n"
    "v_mul_f32 v37, v153, v147\n"
    "v_mul_f32 v44, v154, v143\n"
    "buffer_load_dwordx4 v218, s[28:31], s51 offen offset:2048 lds\n"
    "v_mfma_f32_16x16x32_bf16 v[208:211], v[56:59], v[196:199], 0\n"
    "v_mul_f32 v45, v155, v147\n"
    "v_fmac_f32 v20, v150, v142\n"
    "v_fmac_f32 v21, v151, v146\n"
    "v_fmac_f32 v28, v152, v142\n"
    "v_fmac_f32 v29, v153, v146\n"
    "v_fmac_f32 v36, v154, v142\n"
    "buffer_load_dwordx4 v219, s[28:31], s51 offen offset:3072 lds\n"
    "v_mfma_f32_16x16x32_bf16 v[212:215], v[60:63], v[196:199], 0\n"
    "v_fmac_f32 v37, v155, v146\n"
    "v_fmac_f32 v44, v156, v142\n"
    "v_fmac_f32 v45, v157, v146\n"
    "v_fmac_f32 v20, v152, v141\n"
    "v_fmac_f32 v21, v153, v145\n"
    "v_fmac_f32 v28, v154, v141\n"
    "s_add_u32 m0, s61, 8192\n"
    "s_add_u32 s51, s52, 0\n"
    "buffer_load_dwordx4 v224, s[32:35], s51 offen lds\n"
    "v_mfma_f32_16x16x32_bf16 v[200:203], v[124:127], v[196:199], v[200:203]\n"
    "v_fmac_f32 v29, v155, v145\n"
    "v_fmac_f32 v36, v156, v141\n"
    "v_fmac_f32 v37, v157, v145\n"
    "v_fmac_f32 v44, v158, v141\n"
    "v_fmac_f32 v45, v159, v145\n"
    "v_fmac_f32 v20, v154, v140\n"
    "buffer_load_dwordx4 v225, s[32:35], s51 offen offset:1024 lds\n"
    "v_mfma_f32_16x16x32_bf16 v[204:207], v[128:131], v[196:199], v[204:207]\n"
    "v_fmac_f32 v21, v155, v144\n"
    "v_fmac_f32 v28, v156, v140\n"
    "v_fmac_f32 v29, v157, v144\n"
    "v_fmac_f32 v36, v158, v140\n"
    "v_fmac_f32 v37, v159, v144\n"
    "v_fmac_f32 v44, v160, v140\n"
    "s_add_u32 m0, s61, 32768\n"
    "s_add_u32 s51, s52, 128\n"
    "buffer_load_dwordx4 v224, s[32:35], s51 offen lds\n"
    "v_mfma_f32_16x16x32_bf16 v[208:211], v[132:135], v[196:199], v[208:211]\n"
    "v_fmac_f32 v45, v161, v144\n"
    "v_lshlrev_b32 v148, 16, v67\n"
    "v_and_b32 v149, v164, v67\n"
    "v_lshlrev_b32 v150, 16, v71\n"
    "v_and_b32 v151, v164, v71\n"
    "v_lshlrev_b32 v152, 16, v75\n"
    "buffer_load_dwordx4 v225, s[32:35], s51 offen offset:1024 lds\n"
    "v_mfma_f32_16x16x32_bf16 v[212:215], v[136:139], v[196:199], v[212:215]\n"
    "s_setprio 0\n"
    "v_and_b32 v153, v164, v75\n"
    "v_lshlrev_b32 v154, 16, v79\n"
    "v_and_b32 v155, v164, v79\n"
    "v_lshlrev_b32 v156, 16, v83\n"
    "v_and_b32 v157, v164, v83\n"
    "v_lshlrev_b32 v158, 16, v87\n"
    "s_add_u32 m0, s61, 57344\n"
    "s_add_u32 s51, s52, 256\n"
    "buffer_load_dwordx4 v224, s[32:35], s51 offen lds\n"
    "s_mov_b32 vcc_lo, 131074\n"
    "s_mov_b32 vcc_hi, 131074\n"
    "v_cndmask_b32 v200, v200, v204, vcc\n"
    "v_cndmask_b32 v201, v201, v205, vcc\n"
    "v_cndmask_b32 v202, v202, v206, vcc\n"
    "v_cndmask_b32 v203, v203, v207, vcc\n"
    "v_and_b32 v159, v164, v87\n"
    "v_lshlrev_b32 v160, 16, v91\n"
    "v_and_b32 v161, v164, v91\n"
    "v_lshlrev_b32 v140, 16, v120\n"
    "v_and_b32 v141, v164, v120\n"
    "v_lshlrev_b32 v142, 16, v121\n"
    "buffer_load_dwordx4 v225, s[32:35], s51 offen offset:1024 lds\n"
    "s_mov_b32 vcc_lo, 262148\n"
    "s_mov_b32 vcc_hi, 262148\n"
    "v_cndmask_b32 v200, v200, v208, vcc\n"
    "v_cndmask_b32 v201, v201, v209, vcc\n"
    "v_cndmask_b32 v202, v202, v210, vcc\n"
    "v_cndmask_b32 v203, v203, v211, vcc\n"
    "v_and_b32 v143, v164, v121\n"
    "v_lshlrev_b32 v144, 16, v122\n"
    "v_and_b32 v145, v164, v122\n"
    "v_lshlrev_b32 v146, 16, v123\n"
    "v_and_b32 v147, v164, v123\n"
    "v_mul_f32 v22, v148, v143\n"
    "s_add_u32 m0, s62, 4096\n"
    "s_nop 0\n"
    "buffer_load_dwordx4 v226, s[32:35], s52 offen lds\n"
    "s_mov_b32 vcc_lo, 524296\n"
    "s_mov_b32 vcc_hi, 524296\n"
    "v_cndmask_b32 v200, v200, v212, vcc\n"
    "v_cndmask_b32 v201, v201, v213, vcc\n"
    "v_cndmask_b32 v202, v202, v214, vcc\n"
    "v_cndmask_b32 v203, v203, v215, vcc\n"
    "v_mul_f32 v23, v149, v147\n"
    "v_mul_f32 v30, v150, v143\n"
    "v_mul_f32 v31, v151, v147\n"
    "v_mul_f32 v38, v152, v143\n"
    "v_mul_f32 v39, v153, v147\n"
    "v_mul_f32 v46, v154, v143\n"
    "s_mov_b32 exec_lo, 0x000f000f\n"
    "s_mov_b32 exec_hi, 0x000f000f\n"
    "buffer_store_dwordx4 v[200:203], v238, s[24:27], 0 offen sc1\n"
    "s_mov_b64 exec, -1\n"
    "v_readlane_b32 s50, v227, 8\n"
    "s_nop 0\n"
    "s_lshl_b32 s51, s50, 7\n"
    "s_mul_i32 s52, s50, 458752\n"
    "s_add_u32 m0, s60, 32768\n"
    "s_waitcnt vmcnt(12)\n"
    "s_waitcnt lgkmcnt(0)\n"
    "s_barrier\n"
    "buffer_load_dwordx4 v216, s[28:31], s51 offen lds\n"
    "v_mul_f32 v47, v155, v147\n"
    "v_fmac_f32 v22, v150, v142\n"
    "v_fmac_f32 v23, v151, v146\n"
    "v_fmac_f32 v30, v152, v142\n"
    "v_fmac_f32 v31, v153, v146\n"
    "v_fmac_f32 v38, v154, v142\n"
    "buffer_load_dwordx4 v217, s[28:31], s51 offen offset:1024 lds\n"
    "v_fmac_f32 v39, v155, v146\n"
    "v_fmac_f32 v46, v156, v142\n"
    "v_fmac_f32 v47, v157, v146\n"
    "v_fmac_f32 v22, v152, v141\n"
    "v_fmac_f32 v23, v153, v145\n"
    "v_fmac_f32 v30, v154, v141\n"
    "buffer_load_dwordx4 v218, s[28:31], s51 offen offset:2048 lds\n"
    "v_fmac_f32 v31, v155, v145\n"
    "v_fmac_f32 v38, v156, v141\n"
    "v_fmac_f32 v39, v157, v145\n"
    "v_fmac_f32 v46, v158, v141\n"
    "v_fmac_f32 v47, v159, v145\n"
    "v_fmac_f32 v22, v154, v140\n"
    "buffer_load_dwordx4 v219, s[28:31], s51 offen offset:3072 lds\n"
    "v_fmac_f32 v23, v155, v144\n"
    "v_fmac_f32 v30, v156, v140\n"
    "v_fmac_f32 v31, v157, v144\n"
    "v_fmac_f32 v38, v158, v140\n"
    "v_fmac_f32 v39, v159, v144\n"
    "v_fmac_f32 v46, v160, v140\n"
    "s_add_u32 m0, s61, 16384\n"
    "s_add_u32 s51, s52, 0\n"
    "buffer_load_dwordx4 v224, s[32:35], s51 offen lds\n"
    "v_fmac_f32 v47, v161, v144\n"
    "v_lshlrev_b32 v140, 16, v92\n"
    "v_and_b32 v141, v164, v92\n"
    "v_lshlrev_b32 v142, 16, v93\n"
    "v_and_b32 v143, v164, v93\n"
    "buffer_load_dwordx4 v225, s[32:35], s51 offen offset:1024 lds\n"
    "v_lshlrev_b32 v144, 16, v94\n"
    "v_and_b32 v145, v164, v94\n"
    "v_lshlrev_b32 v146, 16, v95\n"
    "v_and_b32 v147, v164, v95\n"
    "v_mul_f32 v16, v16, v140\n"
    "v_mul_f32 v17, v17, v141\n"
    "s_add_u32 m0, s61, 40960\n"
    "s_add_u32 s51, s52, 128\n"
    "buffer_load_dwordx4 v224, s[32:35], s51 offen lds\n"
    "v_mul_f32 v18, v18, v142\n"
    "v_mul_f32 v19, v19, v143\n"
    "v_mul_f32 v20, v20, v144\n"
    "v_mul_f32 v21, v21, v145\n"
    "v_mul_f32 v22, v22, v146\n"
    "v_mul_f32 v23, v23, v147\n"
    "buffer_load_dwordx4 v225, s[32:35], s51 offen offset:1024 lds\n"
    "v_lshlrev_b32 v140, 16, v96\n"
    "v_and_b32 v141, v164, v96\n"
    "v_lshlrev_b32 v142, 16, v97\n"
    "v_and_b32 v143, v164, v97\n"
    "v_lshlrev_b32 v144, 16, v98\n"
    "v_and_b32 v145, v164, v98\n"
    "s_add_u32 m0, s61, 65536\n"
    "s_add_u32 s51, s52, 256\n"
    "buffer_load_dwordx4 v224, s[32:35], s51 offen lds\n"
    "v_lshlrev_b32 v146, 16, v99\n"
    "v_and_b32 v147, v164, v99\n"
    "v_mul_f32 v24, v24, v140\n"
    "v_mul_f32 v25, v25, v141\n"
    "v_mul_f32 v26, v26, v142\n"
    "v_mul_f32 v27, v27, v143\n"
    "buffer_load_dwordx4 v225, s[32:35], s51 offen offset:1024 lds\n"
    "v_mul_f32 v28, v28, v144\n"
    "v_mul_f32 v29, v29, v145\n"
    "v_mul_f32 v30, v30, v146\n"
    "v_mul_f32 v31, v31, v147\n"
    "v_cvt_pk_bf16_f32 v176, v16, v24\n"
    "v_cvt_pk_bf16_f32 v180, v17, v25\n"
    "s_add_u32 m0, s62, 8192\n"
    "s_nop 0\n"
    "buffer_load_dwordx4 v226, s[32:35], s52 offen lds\n"
    "v_cvt_pk_bf16_f32 v184, v18, v26\n"
    "v_cvt_pk_bf16_f32 v188, v19, v27\n"
    "v_cvt_pk_bf16_f32 v178, v20, v28\n"
    "v_cvt_pk_bf16_f32 v182, v21, v29\n"
    "v_cvt_pk_bf16_f32 v186, v22, v30\n"
    "v_cvt_pk_bf16_f32 v190, v23, v31\n"
    "v_readlane_b32 s50, v227, 9\n"
    "s_nop 0\n"
    "s_lshl_b32 s51, s50, 7\n"
    "s_mul_i32 s52, s50, 458752\n"
    "s_add_u32 m0, s60, 0\n"
    "s_waitcnt vmcnt(12)\n"
    "s_waitcnt lgkmcnt(0)\n"
    "s_barrier\n"
    "buffer_load_dwordx4 v216, s[28:31], s51 offen lds\n"
    "v_lshlrev_b32 v140, 16, v100\n"
    "v_and_b32 v141, v164, v100\n"
    "v_lshlrev_b32 v142, 16, v101\n"
    "v_and_b32 v143, v164, v101\n"
    "v_lshlrev_b32 v144, 16, v102\n"
    "v_and_b32 v145, v164, v102\n"
    "buffer_load_dwordx4 v217, s[28:31], s51 offen offset:1024 lds\n"
    "v_lshlrev_b32 v146, 16, v103\n"
    "v_and_b32 v147, v164, v103\n"
    "v_mul_f32 v32, v32, v140\n"
    "v_mul_f32 v33, v33, v141\n"
    "v_mul_f32 v34, v34, v142\n"
    "v_mul_f32 v35, v35, v143\n"
    "buffer_load_dwordx4 v218, s[28:31], s51 offen offset:2048 lds\n"
    "v_mul_f32 v36, v36, v144\n"
    "v_mul_f32 v37, v37, v145\n"
    "v_mul_f32 v38, v38, v146\n"
    "v_mul_f32 v39, v39, v147\n"
    "v_lshlrev_b32 v140, 16, v104\n"
    "v_and_b32 v141, v164, v104\n"
    "buffer_load_dwordx4 v219, s[28:31], s51 offen offset:3072 lds\n"
    "v_lshlrev_b32 v142, 16, v105\n"
    "v_and_b32 v143, v164, v105\n"
    "v_lshlrev_b32 v144, 16, v106\n"
    "v_and_b32 v145, v164, v106\n"
    "v_lshlrev_b32 v146, 16, v107\n"
    "v_and_b32 v147, v164, v107\n"
    "s_add_u32 m0, s61, 0\n"
    "s_add_u32 s51, s52, 0\n"
    "buffer_load_dwordx4 v224, s[32:35], s51 offen lds\n"
    "v_mul_f32 v40, v40, v140\n"
    "v_mul_f32 v41, v41, v141\n"
    "v_mul_f32 v42, v42, v142\n"
    "v_mul_f32 v43, v43, v143\n"
    "v_mul_f32 v44, v44, v144\n"
    "v_mul_f32 v45, v45, v145\n"
    "buffer_load_dwordx4 v225, s[32:35], s51 offen offset:1024 lds\n"
    "v_mul_f32 v46, v46, v146\n"
    "v_mul_f32 v47, v47, v147\n"
    "v_cvt_pk_bf16_f32 v177, v32, v40\n"
    "v_cvt_pk_bf16_f32 v181, v33, v41\n"
    "v_cvt_pk_bf16_f32 v185, v34, v42\n"
    "v_cvt_pk_bf16_f32 v189, v35, v43\n"
    "s_add_u32 m0, s61, 24576\n"
    "s_add_u32 s51, s52, 128\n"
    "buffer_load_dwordx4 v224, s[32:35], s51 offen lds\n"
    "v_cvt_pk_bf16_f32 v179, v36, v44\n"
    "v_cvt_pk_bf16_f32 v183, v37, v45\n"
    "v_cvt_pk_bf16_f32 v187, v38, v46\n"
    "v_cvt_pk_bf16_f32 v191, v39, v47\n"
    "v_mov_b32_dpp v140, v178 row_ror:8 row_mask:0xf bank_mask:0xf\n"
    "v_mov_b32_dpp v141, v179 row_ror:8 row_mask:0xf bank_mask:0xf\n"
    "buffer_load_dwordx4 v225, s[32:35], s51 offen offset:1024 lds\n"
    "v_mov_b32_dpp v142, v182 row_ror:8 row_mask:0xf bank_mask:0xf\n"
    "v_mov_b32_dpp v143, v183 row_ror:8 row_mask:0xf bank_mask:0xf\n"
    "v_mov_b32_dpp v144, v186 row_ror:8 row_mask:0xf bank_mask:0xf\n"
    "v_mov_b32_dpp v145, v187 row_ror:8 row_mask:0xf bank_mask:0xf\n"
    "v_mov_b32_dpp v146, v190 row_ror:8 row_mask:0xf bank_mask:0xf\n"
    "v_mov_b32_dpp v147, v191 row_ror:8 row_mask:0xf bank_mask:0xf\n"
    "s_add_u32 m0, s61, 49152\n"
    "s_add_u32 s51, s52, 256\n"
    "buffer_load_dwordx4 v224, s[32:35], s51 offen lds\n"
    "v_mov_b32_dpp v178, v176 row_ror:8 row_mask:0xf bank_mask:0x3\n"
    "v_mov_b32_dpp v179, v177 row_ror:8 row_mask:0xf bank_mask:0x3\n"
    "v_mov_b32_dpp v182, v180 row_ror:8 row_mask:0xf bank_mask:0x3\n"
    "v_mov_b32_dpp v183, v181 row_ror:8 row_mask:0xf bank_mask:0x3\n"
    "v_mov_b32_dpp v186, v184 row_ror:8 row_mask:0xf bank_mask:0x3\n"
    "v_mov_b32_dpp v187, v185 row_ror:8 row_mask:0xf bank_mask:0x3\n"
    "buffer_load_dwordx4 v225, s[32:35], s51 offen offset:1024 lds\n"
    "v_mov_b32_dpp v190, v188 row_ror:8 row_mask:0xf bank_mask:0x3\n"
    "v_mov_b32_dpp v191, v189 row_ror:8 row_mask:0xf bank_mask:0x3\n"
    "v_mov_b32_dpp v176, v140 quad_perm:[0,1,2,3] row_mask:0xf bank_mask:0xc\n"
    "v_mov_b32_dpp v177, v141 quad_perm:[0,1,2,3] row_mask:0xf bank_mask:0xc\n"
    "v_mov_b32_dpp v180, v142 quad_perm:[0,1,2,3] row_mask:0xf bank_mask:0xc\n"
    "v_mov_b32_dpp v181, v143 quad_perm:[0,1,2,3] row_mask:0xf bank_mask:0xc\n"
    "s_add_u32 m0, s62, 0\n"
    "s_nop 0\n"
    "buffer_load_dwordx4 v226, s[32:35], s52 offen lds\n"
    "v_mov_b32_dpp v184, v144 quad_perm:[0,1,2,3] row_mask:0xf bank_mask:0xc\n"
    "v_mov_b32_dpp v185, v145 quad_perm:[0,1,2,3] row_mask:0xf bank_mask:0xc\n"
    "v_mov_b32_dpp v188, v146 quad_perm:[0,1,2,3] row_mask:0xf bank_mask:0xc\n"
    "v_mov_b32_dpp v189, v147 quad_perm:[0,1,2,3] row_mask:0xf bank_mask:0xc\n"
    "buffer_load_dword v252, v252, s[24:27], 0 offen sc1\n"
    "s_mov_b32 s53, 0\n"
    "buffer_store_dwordx4 v[176:179], v242, s[24:27], s53 offen sc1\n"
    "s_mov_b32 s53, 16384\n"
    "buffer_store_dwordx4 v[180:183], v242, s[24:27], s53 offen sc1\n"
    "s_mov_b32 s53, 32768\n"
    "buffer_store_dwordx4 v[184:187], v242, s[24:27], s53 offen sc1\n"
    "s_mov_b32 s53, 49152\n"
    "buffer_store_dwordx4 v[188:191], v242, s[24:27], s53 offen sc1\n"
    "v_readlane_b32 s50, v227, 10\n"
    "s_nop 0\n"
    "s_lshl_b32 s51, s50, 7\n"
    "s_mul_i32 s52, s50, 458752\n"
    "s_add_u32 m0, s60, 16384\n"
    "s_waitcnt vmcnt(16)\n"
    "s_waitcnt lgkmcnt(0)\n"
    "s_barrier\n"
    "buffer_load_dwordx4 v216, s[28:31], s51 offen lds\n"
    "buffer_load_dwordx4 v217, s[28:31], s51 offen offset:1024 lds\n"
    "buffer_load_dwordx4 v218, s[28:31], s51 offen offset:2048 lds\n"
    "buffer_load_dwordx4 v219, s[28:31], s51 offen offset:3072 lds\n"
    "s_add_u32 m0, s61, 8192\n"
    "s_add_u32 s51, s52, 0\n"
    "buffer_load_dwordx4 v224, s[32:35], s51 offen lds\n"
    "buffer_load_dwordx4 v225, s[32:35], s51 offen offset:1024 lds\n"
    "s_add_u32 m0, s61, 32768\n"
    "s_add_u32 s51, s52, 128\n"
    "buffer_load_dwordx4 v224, s[32:35], s51 offen lds\n"
    "buffer_load_dwordx4 v225, s[32:35], s51 offen offset:1024 lds\n"
    "s_add_u32 m0, s61, 57344\n"
    "s_add_u32 s51, s52, 256\n"
    "buffer_load_dwordx4 v224, s[32:35], s51 offen lds\n"
    "buffer_load_dwordx4 v225, s[32:35], s51 offen offset:1024 lds\n"
    "s_add_u32 m0, s62, 4096\n"
    "s_nop 0\n"
    "buffer_load_dwordx4 v226, s[32:35], s52 offen lds\n"
    "s_waitcnt vmcnt(11)\n"
    "v_readlane_b32 s50, v227, 11\n"
    "s_nop 0\n"
    "s_lshl_b32 s51, s50, 7\n"
    "s_mul_i32 s52, s50, 458752\n"
    "s_add_u32 m0, s60, 32768\n"
    "s_waitcnt lgkmcnt(0)\n"
    "s_barrier\n"
    "buffer_load_dwordx4 v216, s[28:31], s51 offen lds\n"
    "buffer_load_dwordx4 v217, s[28:31], s51 offen offset:1024 lds\n"
    "buffer_load_dwordx4 v218, s[28:31], s51 offen offset:2048 lds\n"
    "buffer_load_dwordx4 v219, s[28:31], s51 offen offset:3072 lds\n"
    "s_add_u32 m0, s61, 16384\n"
    "s_add_u32 s51, s52, 0\n"
    "buffer_load_dwordx4 v224, s[32:35], s51 offen lds\n"
    "buffer_load_dwordx4 v225, s[32:35], s51 offen offset:1024 lds\n"
    "s_add_u32 m0, s61, 40960\n"
    "s_add_u32 s51, s52, 128\n"
    "buffer_load_dwordx4 v224, s[32:35], s51 offen lds\n"
    "buffer_load_dwordx4 v225, s[32:35], s51 offen offset:1024 lds\n"
    "s_add_u32 m0, s61, 65536\n"
    "s_add_u32 s51, s52, 256\n"
    "buffer_load_dwordx4 v224, s[32:35], s51 offen lds\n"
    "buffer_load_dwordx4 v225, s[32:35], s51 offen offset:1024 lds\n"
    "s_add_u32 m0, s62, 8192\n"
    "s_nop 0\n"
    "buffer_load_dwordx4 v226, s[32:35], s52 offen lds\n"
    "s_cmp_lg_u32 s40, 0\n"
    "s_cbranch_scc1 Lw07_%=\n"
    "v_cmp_eq_u32 vcc, v251, v252\n"
    "s_cbranch_vccnz Lh2_%=\n"
    "Lb3_%=:\n"
    "Lw07_%=:\n"
    "s_and_b32 s53, s2, 63\n"
    "s_lshl_b32 s53, s53, 7\n"
    "s_mov_b64 exec, 1\n"
    "v_mov_b32 v247, 1\n"
    "buffer_atomic_add v247, v253, s[24:27], s53 offen sc1\n"
    "s_mov_b64 exec, -1\n"
    "v_readlane_b32 s50, v227, 12\n"
    "s_nop 0\n"
    "s_lshl_b32 s51, s50, 7\n"
    "s_mul_i32 s52, s50, 458752\n"
    "s_add_u32 m0, s60, 0\n"
    "s_waitcnt vmcnt(12)\n"
    "s_waitcnt lgkmcnt(0)\n"
    "s_barrier\n"
    "buffer_load_dwordx4 v216, s[28:31], s51 offen lds\n"
    "buffer_load_dwordx4 v217, s[28:31], s51 offen offset:1024 lds\n"
    "buffer_load_dwordx4 v218, s[28:31], s51 offen offset:2048 lds\n"
    "buffer_load_dwordx4 v219, s[28:31], s51 offen offset:3072 lds\n"
    "s_add_u32 m0, s61, 0\n"
    "s_add_u32 s51, s52, 0\n"
    "buffer_load_dwordx4 v224, s[32:35], s51 offen lds\n"
    "buffer_load_dwordx4 v225, s[32:35], s51 offen offset:1024 lds\n"
    "s_add_u32 m0, s61, 24576\n"
    "s_add_u32 s51, s52, 128\n"
    "buffer_load_dwordx4 v224, s[32:35], s51 offen lds\n"
    "buffer_load_dwordx4 v225, s[32:35], s51 offen offset:1024 lds\n"
    "s_add_u32 m0, s61, 49152\n"
    "s_add_u32 s51, s52, 256\n"
    "buffer_load_dwordx4 v224, s[32:35], s51 offen lds\n"
    "buffer_load_dwordx4 v225, s[32:35], s51 offen offset:1024 lds\n"
    "s_add_u32 m0, s62, 0\n"
    "s_nop 0\n"
    "buffer_load_dwordx4 v226, s[32:35], s52 offen lds\n"
    "v_readlane_b32 s50, v227, 13\n"
    "s_nop 0\n"
    "s_lshl_b32 s51, s50, 7\n"
    "s_mul_i32 s52, s50, 458752\n"
    "s_add_u32 m0, s60, 16384\n"
    "s_waitcnt vmcnt(12)\n"
    "s_waitcnt lgkmcnt(0)\n"
    "s_barrier\n"
    "buffer_load_dwordx4 v216, s[28:31], s51 offen lds\n"
    "buffer_load_dwordx4 v217, s[28:31], s51 offen offset:1024 lds\n"
    "buffer_load_dwordx4 v218, s[28:31], s51 offen offset:2048 lds\n"
    "buffer_load_dwordx4 v219, s[28:31], s51 offen offset:3072 lds\n"
    "s_add_u32 m0, s61, 8192\n"
    "s_add_u32 s51, s52, 0\n"
    "buffer_load_dwordx4 v224, s[32:35], s51 offen lds\n"
    "buffer_load_dwordx4 v225, s[32:35], s51 offen offset:1024 lds\n"
    "s_add_u32 m0, s61, 32768\n"
    "s_add_u32 s51, s52, 128\n"
    "buffer_load_dwordx4 v224, s[32:35], s51 offen lds\n"
    "buffer_load_dwordx4 v225, s[32:35], s51 offen offset:1024 lds\n"
    "s_add_u32 m0, s61, 57344\n"
    "s_add_u32 s51, s52, 256\n"
    "buffer_load_dwordx4 v224, s[32:35], s51 offen lds\n"
    "buffer_load_dwordx4 v225, s[32:35], s51 offen offset:1024 lds\n"
    "s_add_u32 m0, s62, 4096\n"
    "s_nop 0\n"
    "buffer_load_dwordx4 v226, s[32:35], s52 offen lds\n"
    "v_min_u32 v247, 55, v255\n"
    "v_lshrrev_b32 v228, 3, v247\n"
    "v_and_b32 v247, 7, v247\n"
    "v_lshlrev_b32 v247, 7, v247\n"
    "v_lshl_add_u32 v247, v228, 11, v247\n"
    "v_add_u32 v247, s44, v247\n"
    "global_load_dword v228, v247, s[18:19]\n"
    "v_readlane_b32 s50, v227, 14\n"
    "s_nop 0\n"
    "s_lshl_b32 s51, s50, 7\n"
    "s_mul_i32 s52, s50, 458752\n"
    "s_add_u32 m0, s60, 32768\n"
    "s_waitcnt vmcnt(12)\n"
    "s_waitcnt lgkmcnt(0)\n"
    "s_barrier\n"
    "buffer_load_dwordx4 v216, s[28:31], s51 offen lds\n"
    "buffer_load_dwordx4 v217, s[28:31], s51 offen offset:1024 lds\n"
    "buffer_load_dwordx4 v218, s[28:31], s51 offen offset:2048 lds\n"
    "buffer_load_dwordx4 v219, s[28:31], s51 offen offset:3072 lds\n"
    "s_add_u32 m0, s61, 16384\n"
    "s_add_u32 s51, s52, 0\n"
    "buffer_load_dwordx4 v224, s[32:35], s51 offen lds\n"
    "buffer_load_dwordx4 v225, s[32:35], s51 offen offset:1024 lds\n"
    "s_add_u32 m0, s61, 40960\n"
    "s_add_u32 s51, s52, 128\n"
    "buffer_load_dwordx4 v224, s[32:35], s51 offen lds\n"
    "buffer_load_dwordx4 v225, s[32:35], s51 offen offset:1024 lds\n"
    "s_add_u32 m0, s61, 65536\n"
    "s_add_u32 s51, s52, 256\n"
    "buffer_load_dwordx4 v224, s[32:35], s51 offen lds\n"
    "buffer_load_dwordx4 v225, s[32:35], s51 offen offset:1024 lds\n"
    "s_add_u32 m0, s62, 8192\n"
    "s_nop 0\n"
    "buffer_load_dwordx4 v226, s[32:35], s52 offen lds\n"
    "v_readlane_b32 s50, v227, 15\n"
    "s_nop 0\n"
    "s_lshl_b32 s51, s50, 7\n"
    "s_mul_i32 s52, s50, 458752\n"
    "s_add_u32 m0, s60, 0\n"
    "s_waitcnt vmcnt(12)\n"
    "s_waitcnt lgkmcnt(0)\n"
    "s_barrier\n"
    "buffer_load_dword v252, v253, s[24:27], 0 offen sc1\n"
    "buffer_load_dwordx4 v216, s[28:31], s51 offen lds\n"
    "buffer_load_dwordx4 v217, s[28:31], s51 offen offset:1024 lds\n"
    "buffer_load_dwordx4 v218, s[28:31], s51 offen offset:2048 lds\n"
    "buffer_load_dwordx4 v219, s[28:31], s51 offen offset:3072 lds\n"
    "s_add_u32 m0, s61, 0\n"
    "s_add_u32 s51, s52, 0\n"
    "buffer_load_dwordx4 v224, s[32:35], s51 offen lds\n"
    "buffer_load_dwordx4 v225, s[32:35], s51 offen offset:1024 lds\n"
    "s_add_u32 m0, s61, 24576\n"
    "s_add_u32 s51, s52, 128\n"
    "buffer_load_dwordx4 v224, s[32:35], s51 offen lds\n"
    "buffer_load_dwordx4 v225, s[32:35], s51 offen offset:1024 lds\n"
    "s_add_u32 m0, s61, 49152\n"
    "s_add_u32 s51, s52, 256\n"
    "buffer_load_dwordx4 v224, s[32:35], s51 offen lds\n"
    "buffer_load_dwordx4 v225, s[32:35], s51 offen offset:1024 lds\n"
    "s_add_u32 m0, s62, 0\n"
    "s_nop 0\n"
    "buffer_load_dwordx4 v226, s[32:35], s52 offen lds\n"
    "s_waitcnt vmcnt(11)\n"
    "s_cmp_lg_u32 s40, 0\n"
    "s_cbranch_scc1 Lw013_%=\n"
    "v_cmp_gt_u32 vcc, 4, v252\n"
    "s_cbranch_vccnz Lh8_%=\n"
    "Lb9_%=:\n"
    "Lw013_%=:\n"
    "s_waitcnt lgkmcnt(0)\n"
    "s_barrier\n"
    "buffer_load_dwordx4 v[140:143], v243, s[24:27], 0 offen offset:0 sc1\n"
    "buffer_load_dwordx4 v[144:147], v243, s[24:27], 0 offen offset:1280 sc1\n"
    "buffer_load_dwordx4 v[148:151], v243, s[24:27], 0 offen offset:2560 sc1\n"
    "buffer_load_dwordx4 v[152:155], v243, s[24:27], 0 offen offset:3840 sc1\n"
    "s_lshl_b32 s54, s49, 10\n"
    "s_add_u32 m0, s54, 152192\n"
    "s_lshl1_add_u32 s53, s41, s49\n"
    "s_lshl_b32 s53, s53, 11\n"
    "s_add_u32 s53, s53, 98304\n"
    "v_lshlrev_b32 v247, 1, v249\n"
    "buffer_load_dwordx4 v247, s[24:27], s53 offen sc1 lds\n"
    "s_mov_b32 m0, s63\n"
    "s_lshl_b32 s53, s70, 12\n"
    "s_add_u32 s53, s53, 524288\n"
    "buffer_load_dwordx4 v249, s[24:27], s53 offen sc1 lds\n"
    "buffer_load_dwordx4 v249, s[24:27], s53 offen offset:1024 sc1 lds\n"
    "buffer_load_dwordx4 v249, s[24:27], s53 offen offset:2048 sc1 lds\n"
    "buffer_load_dwordx4 v249, s[24:27], s53 offen offset:3072 sc1 lds\n"
    "v_lshrrev_b32 v10, 1, v7\n"
    "v_and_b32 v11, 1, v7\n"
    "v_and_b32 v13, 15, v255\n"
    "v_lshl_add_u32 v14, v11, 1, v10\n"
    "v_lshl_add_u32 v238, v14, 3, v8\n"
    "v_lshlrev_b32 v238, 8, v238\n"
    "v_lshl_add_u32 v238, v9, 3, v238\n"
    "s_lshl_b32 s54, s49, 13\n"
    "s_add_u32 s54, s54, 135424\n"
    "v_add_u32 v238, s54, v238\n"
    "v_lshl_add_u32 v12, v11, 2, v8\n"
    "s_lshl_b32 s54, s48, 2\n"
    "v_xor_b32 v12, s54, v12\n"
    "v_lshl_add_u32 v238, v12, 5, v238\n"
    "s_lshl_b32 s54, s49, 2\n"
    "v_add_u32 v14, s54, v14\n"
    "v_lshrrev_b32 v12, 1, v13\n"
    "v_xor_b32 v12, v12, v14\n"
    "v_lshlrev_b32 v12, 4, v12\n"
    "v_lshl_add_u32 v12, v13, 7, v12\n"
    "s_lshl_b32 s54, s48, 13\n"
    "s_add_u32 s54, s54, 65536\n"
    "v_add_u32 v239, s54, v12\n"
    "s_lshl_b32 s54, s48, 10\n"
    "s_add_u32 s54, s54, 152192\n"
    "v_lshl_add_u32 v240, v13, 4, s54\n"
    "v_lshlrev_b32 v12, 4, v14\n"
    "v_lshl_add_u32 v12, v13, 11, v12\n"
    "s_lshl1_add_u32 s55, s41, s48\n"
    "s_lshl_b32 s55, s55, 17\n"
    "s_lshl1_add_u32 s56, s46, s45\n"
    "s_lshl_b32 s56, s56, 7\n"
    "s_add_u32 s55, s55, s56\n"
    "v_add_u32 v241, s55, v12\n"
    "s_lshl4_add_u32 s55, s41, s42\n"
    "s_lshl2_add_u32 s55, s55, s40\n"
    "s_lshl_b32 s55, s55, 12\n"
    "s_xor_b32 s56, s55, 131072\n"
    "s_add_u32 s55, s55, 19398656\n"
    "s_add_u32 s56, s56, 19398656\n"
    "s_mov_b32 s36, s22\n"
    "s_and_b32 s37, s23, 0xffff\n"
    "s_mov_b32 s38, 4194304\n"
    "s_mov_b32 s39, 0x20000\n"
    "s_mov_b32 m0, s43\n"
    "s_lshl_b32 s53, s3, 12\n"
    "v_add_u32 v247, s53, v249\n"
    "global_load_lds_dwordx4 v247, s[12:13] offset:0\n"
    "global_load_lds_dwordx4 v247, s[12:13] offset:1024\n"
    "global_load_lds_dwordx4 v247, s[12:13] offset:2048\n"
    "global_load_lds_dwordx4 v247, s[12:13] offset:3072\n"
    "s_waitcnt vmcnt(13)\n"
    "s_waitcnt lgkmcnt(0)\n"
    "s_barrier\n"
    "s_mov_b32 s32, s18\n"
    "s_and_b32 s33, s19, 0xffff\n"
    "s_mov_b32 s34, 7340032\n"
    "s_mov_b32 s35, 0x20000\n"
    "s_lshl_b32 s53, s40, 2\n"
    "s_lshr_b32 s58, 0x5320, s53\n"
    "s_and_b32 s58, s58, 15\n"
    "s_lshr_b32 s62, 0x7641, s53\n"
    "s_and_b32 s62, s62, 15\n"
    "s_lshl_b32 s60, s58, 13\n"
    "s_lshl_b32 s61, s62, 13\n"
    "s_waitcnt vmcnt(4)\n"
    "v_add_f32 v140, v140, v144\n"
    "v_add_f32 v148, v148, v152\n"
    "v_add_f32 v141, v141, v145\n"
    "v_add_f32 v149, v149, v153\n"
    "v_add_f32 v142, v142, v146\n"
    "v_add_f32 v150, v150, v154\n"
    "v_add_f32 v143, v143, v147\n"
    "v_add_f32 v151, v151, v155\n"
    "v_add_f32 v140, v140, v148\n"
    "v_add_f32 v141, v141, v149\n"
    "v_add_f32 v142, v142, v150\n"
    "v_add_f32 v143, v143, v151\n"
    "v_mov_b32 v144, v140\n"
    "v_mov_b32 v145, v141\n"
    "v_mov_b32 v146, v142\n"
    "v_mov_b32 v147, v143\n"
    "v_permlane32_swap_b32 v140, v144\n"
    "v_permlane32_swap_b32 v141, v145\n"
    "v_permlane32_swap_b32 v142, v146\n"
    "v_permlane32_swap_b32 v143, v147\n"
    "v_add_f32 v140, v140, v144\n"
    "v_add_f32 v141, v141, v145\n"
    "v_add_f32 v142, v142, v146\n"
    "v_add_f32 v143, v143, v147\n"
    "v_mov_b32 v144, v140\n"
    "v_mov_b32 v145, v141\n"
    "v_mov_b32 v146, v142\n"
    "v_mov_b32 v147, v143\n"
    "v_permlane16_swap_b32 v140, v144\n"
    "v_permlane16_swap_b32 v141, v145\n"
    "v_permlane16_swap_b32 v142, v146\n"
    "v_permlane16_swap_b32 v143, v147\n"
    "v_add_f32 v140, v140, v144\n"
    "v_add_f32 v141, v141, v145\n"
    "v_add_f32 v142, v142, v146\n"
    "v_add_f32 v143, v143, v147\n"
    "s_mov_b64 exec, 0xffff\n"
    "ds_write_b128 v244, v[140:143]\n"
    "s_mov_b64 exec, -1\n"
    "s_waitcnt lgkmcnt(0)\n"
    "s_barrier\n"
    "v_mov_b32 v224, 0\n"
    "v_mov_b32 v225, 0\n"
    "v_mov_b32 v226, 0\n"
    "v_mov_b32 v227, 0\n"
    "v_readlane_b32 s50, v234, s58\n"
    "s_add_u32 m0, s60, 65536\n"
    "s_lshl_b32 s51, s50, 9\n"
    "s_add_u32 s51, s51, s45\n"
    "s_lshl_b32 s51, s51, 7\n"
    "buffer_load_dwordx4 v220, s[32:35], s51 offen lds\n"
    "ds_read_b64_tr_b16 v[188:189], v233 offset:16000\n"
    "ds_read_b64_tr_b16 v[190:191], v233 offset:15872\n"
    "ds_read_b64_tr_b16 v[184:185], v233 offset:15488\n"
    "ds_read_b64_tr_b16 v[186:187], v233 offset:15360\n"
    "ds_read_b64_tr_b16 v[180:181], v233 offset:14976\n"
    "ds_read_b64_tr_b16 v[182:183], v233 offset:14848\n"
    "buffer_load_dwordx4 v221, s[32:35], s51 offen offset:1024 lds\n"
    "ds_read_b64_tr_b16 v[176:177], v233 offset:14464\n"
    "ds_read_b64_tr_b16 v[178:179], v233 offset:14336\n"
    "ds_read_b64_tr_b16 v[172:173], v233 offset:13952\n"
    "ds_read_b64_tr_b16 v[174:175], v233 offset:13824\n"
    "ds_read_b64_tr_b16 v[168:169], v233 offset:13440\n"
    "ds_read_b64_tr_b16 v[170:171], v233 offset:13312\n"
    "buffer_load_dwordx4 v222, s[32:35], s51 offen offset:2048 lds\n"
    "ds_read_b64_tr_b16 v[164:165], v233 offset:12928\n"
    "ds_read_b64_tr_b16 v[166:167], v233 offset:12800\n"
    "ds_read_b64_tr_b16 v[160:161], v233 offset:12416\n"
    "ds_read_b64_tr_b16 v[162:163], v233 offset:12288\n"
    "ds_read_b64_tr_b16 v[156:157], v233 offset:11904\n"
    "ds_read_b64_tr_b16 v[158:159], v233 offset:11776\n"
    "buffer_load_dwordx4 v223, s[32:35], s51 offen offset:3072 lds\n"
    "ds_read_b64_tr_b16 v[152:153], v233 offset:11392\n"
    "ds_read_b64_tr_b16 v[154:155], v233 offset:11264\n"
    "ds_read_b64_tr_b16 v[148:149], v233 offset:10880\n"
    "ds_read_b64_tr_b16 v[150:151], v233 offset:10752\n"
    "ds_read_b64_tr_b16 v[144:145], v233 offset:10368\n"
    "ds_read_b64_tr_b16 v[146:147], v233 offset:10240\n"
    "s_add_u32 m0, s60, 69632\n"
    "s_xor_b32 s51, s51, 128\n"
    "buffer_load_dwordx4 v220, s[32:35], s51 offen lds\n"
    "ds_read_b64_tr_b16 v[140:141], v233 offset:9856\n"
    "ds_read_b64_tr_b16 v[142:143], v233 offset:9728\n"
    "ds_read_b64_tr_b16 v[136:137], v233 offset:9344\n"
    "ds_read_b64_tr_b16 v[138:139], v233 offset:9216\n"
    "ds_read_b64_tr_b16 v[132:133], v233 offset:8832\n"
    "ds_read_b64_tr_b16 v[134:135], v233 offset:8704\n"
    "buffer_load_dwordx4 v221, s[32:35], s51 offen offset:1024 lds\n"
    "ds_read_b64_tr_b16 v[128:129], v233 offset:8320\n"
    "ds_read_b64_tr_b16 v[130:131], v233 offset:8192\n"
    "ds_read_b64_tr_b16 v[124:125], v233 offset:7808\n"
    "ds_read_b64_tr_b16 v[126:127], v233 offset:7680\n"
    "ds_read_b64_tr_b16 v[120:121], v233 offset:7296\n"
    "ds_read_b64_tr_b16 v[122:123], v233 offset:7168\n"
    "buffer_load_dwordx4 v222, s[32:35], s51 offen offset:2048 lds\n"
    "ds_read_b64_tr_b16 v[116:117], v233 offset:6784\n"
    "ds_read_b64_tr_b16 v[118:119], v233 offset:6656\n"
    "ds_read_b64_tr_b16 v[112:113], v233 offset:6272\n"
    "ds_read_b64_tr_b16 v[114:115], v233 offset:6144\n"
    "ds_read_b64_tr_b16 v[108:109], v233 offset:5760\n"
    "ds_read_b64_tr_b16 v[110:111], v233 offset:5632\n"
    "buffer_load_dwordx4 v223, s[32:35], s51 offen offset:3072 lds\n"
    "ds_read_b64_tr_b16 v[104:105], v233 offset:5248\n"
    "ds_read_b64_tr_b16 v[106:107], v233 offset:5120\n"
    "ds_read_b64_tr_b16 v[100:101], v233 offset:4736\n"
    "ds_read_b64_tr_b16 v[102:103], v233 offset:4608\n"
    "ds_read_b64_tr_b16 v[96:97], v233 offset:4224\n"
    "ds_read_b64_tr_b16 v[98:99], v233 offset:4096\n"
    "v_readlane_b32 s50, v234, s62\n"
    "s_add_u32 m0, s61, 65536\n"
    "s_lshl_b32 s51, s50, 9\n"
    "s_add_u32 s51, s51, s45\n"
    "s_lshl_b32 s51, s51, 7\n"
    "buffer_load_dwordx4 v220, s[32:35], s51 offen lds\n"
    "ds_read_b64_tr_b16 v[92:93], v233 offset:3712\n"
    "ds_read_b64_tr_b16 v[94:95], v233 offset:3584\n"
    "ds_read_b64_tr_b16 v[88:89], v233 offset:3200\n"
    "ds_read_b64_tr_b16 v[90:91], v233 offset:3072\n"
    "ds_read_b64_tr_b16 v[84:85], v233 offset:2688\n"
    "ds_read_b64_tr_b16 v[86:87], v233 offset:2560\n"
    "buffer_load_dwordx4 v221, s[32:35], s51 offen offset:1024 lds\n"
    "ds_read_b64_tr_b16 v[80:81], v233 offset:2176\n"
    "ds_read_b64_tr_b16 v[82:83], v233 offset:2048\n"
    "ds_read_b64_tr_b16 v[76:77], v233 offset:1664\n"
    "ds_read_b64_tr_b16 v[78:79], v233 offset:1536\n"
    "ds_read_b64_tr_b16 v[72:73], v233 offset:1152\n"
    "ds_read_b64_tr_b16 v[74:75], v233 offset:1024\n"
    "buffer_load_dwordx4 v222, s[32:35], s51 offen offset:2048 lds\n"
    "ds_read_b64_tr_b16 v[68:69], v233 offset:640\n"
    "ds_read_b64_tr_b16 v[70:71], v233 offset:512\n"
    "ds_read_b64_tr_b16 v[64:65], v233 offset:128\n"
    "ds_read_b64_tr_b16 v[66:67], v233 offset:0\n"
    "s_waitcnt vmcnt(11)\n"
    "ds_read_b64_tr_b16 v[0:1], v236 offset:0\n"
    "buffer_load_dwordx4 v223, s[32:35], s51 offen offset:3072 lds\n"
    "ds_read_b64_tr_b16 v[2:3], v237 offset:32\n"
    "ds_read_b64_tr_b16 v[4:5], v235 offset:256\n"
    "ds_read_b64_tr_b16 v[6:7], v235 offset:288\n"
    "ds_read_b64_tr_b16 v[8:9], v235 offset:512\n"
    "ds_read_b64_tr_b16 v[10:11], v235 offset:544\n"
    "ds_read_b64_tr_b16 v[12:13], v235 offset:768\n"
    "s_add_u32 m0, s61, 69632\n"
    "s_xor_b32 s51, s51, 128\n"
    "buffer_load_dwordx4 v220, s[32:35], s51 offen lds\n"
    "ds_read_b64_tr_b16 v[14:15], v235 offset:800\n"
    "ds_read_b64_tr_b16 v[16:17], v235 offset:1024\n"
    "ds_read_b64_tr_b16 v[18:19], v235 offset:1056\n"
    "ds_read_b64_tr_b16 v[20:21], v235 offset:1280\n"
    "ds_read_b64_tr_b16 v[22:23], v235 offset:1312\n"
    "ds_read_b64_tr_b16 v[24:25], v235 offset:1536\n"
    "buffer_load_dwordx4 v221, s[32:35], s51 offen offset:1024 lds\n"
    "ds_read_b64_tr_b16 v[26:27], v235 offset:1568\n"
    "ds_read_b64_tr_b16 v[28:29], v235 offset:1792\n"
    "ds_read_b64_tr_b16 v[30:31], v235 offset:1824\n"
    "ds_read_b64_tr_b16 v[32:33], v235 offset:2048\n"
    "ds_read_b64_tr_b16 v[34:35], v235 offset:2080\n"
    "ds_read_b64_tr_b16 v[36:37], v235 offset:2304\n"
    "buffer_load_dwordx4 v222, s[32:35], s51 offen offset:2048 lds\n"
    "ds_read_b64_tr_b16 v[38:39], v235 offset:2336\n"
    "ds_read_b64_tr_b16 v[40:41], v235 offset:2560\n"
    "ds_read_b64_tr_b16 v[42:43], v235 offset:2592\n"
    "ds_read_b64_tr_b16 v[44:45], v235 offset:2816\n"
    "ds_read_b64_tr_b16 v[46:47], v235 offset:2848\n"
    "ds_read_b64_tr_b16 v[48:49], v235 offset:3072\n"
    "buffer_load_dwordx4 v223, s[32:35], s51 offen offset:3072 lds\n"
    "ds_read_b64_tr_b16 v[50:51], v235 offset:3104\n"
    "ds_read_b64_tr_b16 v[52:53], v235 offset:3328\n"
    "ds_read_b64_tr_b16 v[54:55], v235 offset:3360\n"
    "ds_read_b64_tr_b16 v[56:57], v235 offset:3584\n"
    "ds_read_b64_tr_b16 v[58:59], v235 offset:3616\n"
    "ds_read_b64_tr_b16 v[60:61], v235 offset:3840\n"
    "ds_read_b64_tr_b16 v[62:63], v235 offset:3872\n"
    "s_waitcnt lgkmcnt(0)\n"
    "s_mov_b32 s59, 1\n"
    "s_mov_b32 s58, 7\n"
    "v_mfma_f32_16x16x32_bf16 v[192:195], v[0:3], v[184:187], 0\n"
    "v_mfma_f32_16x16x32_bf16 v[192:195], v[4:7], v[176:179], v[192:195]\n"
    "v_mfma_f32_16x16x32_bf16 v[192:195], v[8:11], v[168:171], v[192:195]\n"
    "v_mfma_f32_16x16x32_bf16 v[192:195], v[12:15], v[160:163], v[192:195]\n"
    "v_mfma_f32_16x16x32_bf16 v[192:195], v[16:19], v[152:155], v[192:195]\n"
    "v_mfma_f32_16x16x32_bf16 v[192:195], v[20:23], v[144:147], v[192:195]\n"
    "v_mfma_f32_16x16x32_bf16 v[192:195], v[24:27], v[136:139], v[192:195]\n"
    "v_mfma_f32_16x16x32_bf16 v[192:195], v[28:31], v[128:131], v[192:195]\n"
    "v_mfma_f32_16x16x32_bf16 v[192:195], v[32:35], v[120:123], v[192:195]\n"
    "v_mfma_f32_16x16x32_bf16 v[192:195], v[36:39], v[112:115], v[192:195]\n"
    "v_mfma_f32_16x16x32_bf16 v[192:195], v[40:43], v[104:107], v[192:195]\n"
    "v_mfma_f32_16x16x32_bf16 v[192:195], v[44:47], v[96:99], v[192:195]\n"
    "v_mfma_f32_16x16x32_bf16 v[192:195], v[48:51], v[88:91], v[192:195]\n"
    "v_mfma_f32_16x16x32_bf16 v[192:195], v[52:55], v[80:83], v[192:195]\n"
    "v_mfma_f32_16x16x32_bf16 v[192:195], v[56:59], v[72:75], v[192:195]\n"
    "v_mfma_f32_16x16x32_bf16 v[192:195], v[60:63], v[64:67], v[192:195]\n"
    "v_mfma_f32_16x16x32_bf16 v[196:199], v[60:63], v[68:71], 0\n"
    "v_mfma_f32_16x16x32_bf16 v[196:199], v[56:59], v[76:79], v[196:199]\n"
    "v_mfma_f32_16x16x32_bf16 v[196:199], v[52:55], v[84:87], v[196:199]\n"
    "v_mfma_f32_16x16x32_bf16 v[196:199], v[48:51], v[92:95], v[196:199]\n"
    "v_mfma_f32_16x16x32_bf16 v[196:199], v[44:47], v[100:103], v[196:199]\n"
    "v_mfma_f32_16x16x32_bf16 v[196:199], v[40:43], v[108:111], v[196:199]\n"
    "v_mfma_f32_16x16x32_bf16 v[196:199], v[36:39], v[116:119], v[196:199]\n"
    "v_mfma_f32_16x16x32_bf16 v[196:199], v[32:35], v[124:127], v[196:199]\n"
    "v_mfma_f32_16x16x32_bf16 v[196:199], v[28:31], v[132:135], v[196:199]\n"
    "v_mfma_f32_16x16x32_bf16 v[196:199], v[24:27], v[140:143], v[196:199]\n"
    "v_mfma_f32_16x16x32_bf16 v[196:199], v[20:23], v[148:151], v[196:199]\n"
    "v_mfma_f32_16x16x32_bf16 v[196:199], v[16:19], v[156:159], v[196:199]\n"
    "v_mfma_f32_16x16x32_bf16 v[196:199], v[12:15], v[164:167], v[196:199]\n"
    "v_mfma_f32_16x16x32_bf16 v[196:199], v[8:11], v[172:175], v[196:199]\n"
    "v_mfma_f32_16x16x32_bf16 v[196:199], v[4:7], v[180:183], v[196:199]\n"
    "v_mfma_f32_16x16x32_bf16 v[196:199], v[0:3], v[188:191], v[196:199]\n"
    "v_mfma_f32_16x16x32_bf16 v[200:203], v[0:3], v[176:179], 0\n"
    "v_mfma_f32_16x16x32_bf16 v[200:203], v[4:7], v[168:171], v[200:203]\n"
    "v_mfma_f32_16x16x32_bf16 v[200:203], v[8:11], v[160:163], v[200:203]\n"
    "v_mfma_f32_16x16x32_bf16 v[200:203], v[12:15], v[152:155], v[200:203]\n"
    "ds_read_u16_d16_hi v224, v229 offset:15872\n"
    "s_nop 2\n"
    "v_cndmask_b32 v219, v199, v195, s[68:69]\n"
    "v_mfma_f32_16x16x32_bf16 v[200:203], v[16:19], v[144:147], v[200:203]\n"
    "v_cndmask_b32 v218, v198, v194, s[66:67]\n"
    "v_mfma_f32_16x16x32_bf16 v[200:203], v[20:23], v[136:139], v[200:203]\n"
    "v_cndmask_b32 v217, v197, v193, s[64:65]\n"
    "v_add_f32_dpp v218, v219, v218 wave_ror:1 row_mask:0xf bank_mask:0xf\n"
    "v_mfma_f32_16x16x32_bf16 v[200:203], v[24:27], v[128:131], v[200:203]\n"
    "s_nop 0\n"
    "v_add_f32_dpp v217, v218, v217 wave_ror:1 row_mask:0xf bank_mask:0xf\n"
    "v_mfma_f32_16x16x32_bf16 v[200:203], v[28:31], v[120:123], v[200:203]\n"
    "s_nop 0\n"
    "v_add_f32_dpp v216, v217, v196 wave_ror:1 row_mask:0xf bank_mask:0xf\n"
    "s_waitcnt lgkmcnt(0)\n"
    "v_fmac_f32 v216, s71, v224\n"
    "v_mfma_f32_16x16x32_bf16 v[200:203], v[32:35], v[112:115], v[200:203]\n"
    "v_cvt_pk_bf16_f32 v228, v216, v220\n"
    "v_mfma_f32_16x16x32_bf16 v[200:203], v[36:39], v[104:107], v[200:203]\n"
    "ds_write_b16 v229, v228 offset:15872\n"
    "v_mfma_f32_16x16x32_bf16 v[200:203], v[40:43], v[96:99], v[200:203]\n"
    "v_mfma_f32_16x16x32_bf16 v[200:203], v[44:47], v[88:91], v[200:203]\n"
    "v_mfma_f32_16x16x32_bf16 v[200:203], v[48:51], v[80:83], v[200:203]\n"
    "v_mfma_f32_16x16x32_bf16 v[200:203], v[52:55], v[72:75], v[200:203]\n"
    "v_mfma_f32_16x16x32_bf16 v[200:203], v[56:59], v[64:67], v[200:203]\n"
    "v_mfma_f32_16x16x32_bf16 v[204:207], v[56:59], v[68:71], 0\n"
    "v_mfma_f32_16x16x32_bf16 v[204:207], v[52:55], v[76:79], v[204:207]\n"
    "v_mfma_f32_16x16x32_bf16 v[204:207], v[48:51], v[84:87], v[204:207]\n"
    "v_mfma_f32_16x16x32_bf16 v[204:207], v[44:47], v[92:95], v[204:207]\n"
    "v_mfma_f32_16x16x32_bf16 v[204:207], v[40:43], v[100:103], v[204:207]\n"
    "v_mfma_f32_16x16x32_bf16 v[204:207], v[36:39], v[108:111], v[204:207]\n"
    "v_mfma_f32_16x16x32_bf16 v[204:207], v[32:35], v[116:119], v[204:207]\n"
    "v_mfma_f32_16x16x32_bf16 v[204:207], v[28:31], v[124:127], v[204:207]\n"
    "v_mfma_f32_16x16x32_bf16 v[204:207], v[24:27], v[132:135], v[204:207]\n"
    "v_mfma_f32_16x16x32_bf16 v[204:207], v[20:23], v[140:143], v[204:207]\n"
    "v_mfma_f32_16x16x32_bf16 v[204:207], v[16:19], v[148:151], v[204:207]\n"
    "v_mfma_f32_16x16x32_bf16 v[204:207], v[12:15], v[156:159], v[204:207]\n"
    "v_mfma_f32_16x16x32_bf16 v[204:207], v[8:11], v[164:167], v[204:207]\n"
    "v_mfma_f32_16x16x32_bf16 v[204:207], v[4:7], v[172:175], v[204:207]\n"
    "v_mfma_f32_16x16x32_bf16 v[204:207], v[0:3], v[180:183], v[204:207]\n"
    "v_mfma_f32_16x16x32_bf16 v[208:211], v[0:3], v[168:171], 0\n"
    "v_mfma_f32_16x16x32_bf16 v[208:211], v[4:7], v[160:163], v[208:211]\n"
    "v_mfma_f32_16x16x32_bf16 v[208:211], v[8:11], v[152:155], v[208:211]\n"
    "v_mfma_f32_16x16x32_bf16 v[208:211], v[12:15], v[144:147], v[208:211]\n"
    "ds_read_u16_d16_hi v226, v229 offset:14848\n"
    "ds_read_u16_d16_hi v227, v229 offset:15360\n"
    "v_mfma_f32_16x16x32_bf16 v[208:211], v[16:19], v[136:139], v[208:211]\n"
    "s_nop 0\n"
    "v_cndmask_b32 v219, v207, v203, s[68:69]\n"
    "v_mfma_f32_16x16x32_bf16 v[208:211], v[20:23], v[128:131], v[208:211]\n"
    "v_cndmask_b32 v218, v206, v202, s[66:67]\n"
    "v_cndmask_b32 v217, v205, v201, s[64:65]\n"
    "v_mfma_f32_16x16x32_bf16 v[208:211], v[24:27], v[120:123], v[208:211]\n"
    "v_cndmask_b32 v223, v195, v207, s[68:69]\n"
    "v_mfma_f32_16x16x32_bf16 v[208:211], v[28:31], v[112:115], v[208:211]\n"
    "v_cndmask_b32 v222, v194, v206, s[66:67]\n"
    "v_cndmask_b32 v221, v193, v205, s[64:65]\n"
    "v_mfma_f32_16x16x32_bf16 v[208:211], v[32:35], v[104:107], v[208:211]\n"
    "v_add_f32_dpp v218, v219, v218 wave_ror:1 row_mask:0xf bank_mask:0xf\n"
    "v_mfma_f32_16x16x32_bf16 v[208:211], v[36:39], v[96:99], v[208:211]\n"
    "v_add_f32_dpp v222, v223, v222 wave_ror:1 row_mask:0xf bank_mask:0xf\n"
    "v_add_f32_dpp v217, v218, v217 wave_ror:1 row_mask:0xf bank_mask:0xf\n"
    "v_mfma_f32_16x16x32_bf16 v[208:211], v[40:43], v[88:91], v[208:211]\n"
    "v_add_f32_dpp v221, v222, v221 wave_ror:1 row_mask:0xf bank_mask:0xf\n"
    "v_mfma_f32_16x16x32_bf16 v[208:211], v[44:47], v[80:83], v[208:211]\n"
    "v_add_f32_dpp v216, v217, v204 wave_ror:1 row_mask:0xf bank_mask:0xf\n"
    "v_add_f32_dpp v220, v221, v192 wave_ror:1 row_mask:0xf bank_mask:0xf\n"
    "v_mfma_f32_16x16x32_bf16 v[208:211], v[48:51], v[72:75], v[208:211]\n"
    "s_waitcnt lgkmcnt(0)\n"
    "v_fmac_f32 v216, s71, v226\n"
    "v_mfma_f32_16x16x32_bf16 v[208:211], v[52:55], v[64:67], v[208:211]\n"
    "v_fmac_f32 v220, s71, v227\n"
    "v_cvt_pk_bf16_f32 v228, v216, v220\n"
    "v_mfma_f32_16x16x32_bf16 v[212:215], v[52:55], v[68:71], 0\n"
    "ds_write_b16 v229, v228 offset:14848\n"
    "v_mfma_f32_16x16x32_bf16 v[212:215], v[48:51], v[76:79], v[212:215]\n"
    "ds_write_b16_d16_hi v229, v228 offset:15360\n"
    "v_mfma_f32_16x16x32_bf16 v[212:215], v[44:47], v[84:87], v[212:215]\n"
    "v_mfma_f32_16x16x32_bf16 v[212:215], v[40:43], v[92:95], v[212:215]\n"
    "v_mfma_f32_16x16x32_bf16 v[212:215], v[36:39], v[100:103], v[212:215]\n"
    "v_mfma_f32_16x16x32_bf16 v[212:215], v[32:35], v[108:111], v[212:215]\n"
    "v_mfma_f32_16x16x32_bf16 v[212:215], v[28:31], v[116:119], v[212:215]\n"
    "v_mfma_f32_16x16x32_bf16 v[212:215], v[24:27], v[124:127], v[212:215]\n"
    "v_mfma_f32_16x16x32_bf16 v[212:215], v[20:23], v[132:135], v[212:215]\n"
    "v_mfma_f32_16x16x32_bf16 v[212:215], v[16:19], v[140:143], v[212:215]\n"
    "v_mfma_f32_16x16x32_bf16 v[212:215], v[12:15], v[148:151], v[212:215]\n"
    "v_mfma_f32_16x16x32_bf16 v[212:215], v[8:11], v[156:159], v[212:215]\n"
    "v_mfma_f32_16x16x32_bf16 v[212:215], v[4:7], v[164:167], v[212:215]\n"
    "v_mfma_f32_16x16x32_bf16 v[212:215], v[0:3], v[172:175], v[212:215]\n"
    "v_mfma_f32_16x16x32_bf16 v[184:187], v[0:3], v[160:163], 0\n"
    "v_mfma_f32_16x16x32_bf16 v[184:187], v[4:7], v[152:155], v[184:187]\n"
    "v_mfma_f32_16x16x32_bf16 v[184:187], v[8:11], v[144:147], v[184:187]\n"
    "v_mfma_f32_16x16x32_bf16 v[184:187], v[12:15], v[136:139], v[184:187]\n"
    "ds_read_u16_d16_hi v224, v229 offset:13824\n"
    "ds_read_u16_d16_hi v225, v229 offset:14336\n"
    "v_mfma_f32_16x16x32_bf16 v[184:187], v[16:19], v[128:131], v[184:187]\n"
    "s_nop 0\n"
    "v_cndmask_b32 v219, v215, v211, s[68:69]\n"
    "v_mfma_f32_16x16x32_bf16 v[184:187], v[20:23], v[120:123], v[184:187]\n"
    "v_cndmask_b32 v218, v214, v210, s[66:67]\n"
    "v_cndmask_b32 v217, v213, v209, s[64:65]\n"
    "v_mfma_f32_16x16x32_bf16 v[184:187], v[24:27], v[112:115], v[184:187]\n"
    "v_cndmask_b32 v223, v203, v215, s[68:69]\n"
    "v_mfma_f32_16x16x32_bf16 v[184:187], v[28:31], v[104:107], v[184:187]\n"
    "v_cndmask_b32 v222, v202, v214, s[66:67]\n"
    "v_cndmask_b32 v221, v201, v213, s[64:65]\n"
    "v_mfma_f32_16x16x32_bf16 v[184:187], v[32:35], v[96:99], v[184:187]\n"
    "v_add_f32_dpp v218, v219, v218 wave_ror:1 row_mask:0xf bank_mask:0xf\n"
    "v_mfma_f32_16x16x32_bf16 v[184:187], v[36:39], v[88:91], v[184:187]\n"
    "v_add_f32_dpp v222, v223, v222 wave_ror:1 row_mask:0xf bank_mask:0xf\n"
    "v_add_f32_dpp v217, v218, v217 wave_ror:1 row_mask:0xf bank_mask:0xf\n"
    "v_mfma_f32_16x16x32_bf16 v[184:187], v[40:43], v[80:83], v[184:187]\n"
    "v_add_f32_dpp v221, v222, v221 wave_ror:1 row_mask:0xf bank_mask:0xf\n"
    "v_mfma_f32_16x16x32_bf16 v[184:187], v[44:47], v[72:75], v[184:187]\n"
    "v_add_f32_dpp v216, v217, v212 wave_ror:1 row_mask:0xf bank_mask:0xf\n"
    "v_add_f32_dpp v220, v221, v200 wave_ror:1 row_mask:0xf bank_mask:0xf\n"
    "v_mfma_f32_16x16x32_bf16 v[184:187], v[48:51], v[64:67], v[184:187]\n"
    "s_waitcnt lgkmcnt(0)\n"
    "v_fmac_f32 v216, s71, v224\n"
    "v_mfma_f32_16x16x32_bf16 v[188:191], v[48:51], v[68:71], 0\n"
    "v_fmac_f32 v220, s71, v225\n"
    "v_cvt_pk_bf16_f32 v228, v216, v220\n"
    "v_mfma_f32_16x16x32_bf16 v[188:191], v[44:47], v[76:79], v[188:191]\n"
    "ds_write_b16 v229, v228 offset:13824\n"
    "v_mfma_f32_16x16x32_bf16 v[188:191], v[40:43], v[84:87], v[188:191]\n"
    "ds_write_b16_d16_hi v229, v228 offset:14336\n"
    "v_mfma_f32_16x16x32_bf16 v[188:191], v[36:39], v[92:95], v[188:191]\n"
    "v_mfma_f32_16x16x32_bf16 v[188:191], v[32:35], v[100:103], v[188:191]\n"
    "v_mfma_f32_16x16x32_bf16 v[188:191], v[28:31], v[108:111], v[188:191]\n"
    "v_mfma_f32_16x16x32_bf16 v[188:191], v[24:27], v[116:119], v[188:191]\n"
    "v_mfma_f32_16x16x32_bf16 v[188:191], v[20:23], v[124:127], v[188:191]\n"
    "v_mfma_f32_16x16x32_bf16 v[188:191], v[16:19], v[132:135], v[188:191]\n"
    "v_mfma_f32_16x16x32_bf16 v[188:191], v[12:15], v[140:143], v[188:191]\n"
    "v_mfma_f32_16x16x32_bf16 v[188:191], v[8:11], v[148:151], v[188:191]\n"
    "v_mfma_f32_16x16x32_bf16 v[188:191], v[4:7], v[156:159], v[188:191]\n"
    "v_mfma_f32_16x16x32_bf16 v[188:191], v[0:3], v[164:167], v[188:191]\n"
    "s_waitcnt vmcnt(0)\n"
    "v_mov_b32 v248, 1\n"
    "ds_write_b32 v246, v248 offset:16\n"
    "v_mfma_f32_16x16x32_bf16 v[196:199], v[0:3], v[64:67], 0\n"
    "v_mfma_f32_16x16x32_bf16 v[176:179], v[0:3], v[68:71], 0\n"
    "ds_read_u16_d16_hi v226, v229 offset:12800\n"
    "ds_read_u16_d16_hi v227, v229 offset:13312\n"
    "v_mfma_f32_16x16x32_bf16 v[180:183], v[0:3], v[152:155], 0\n"
    "v_cndmask_b32 v219, v191, v187, s[68:69]\n"
    "v_mfma_f32_16x16x32_bf16 v[180:183], v[4:7], v[144:147], v[180:183]\n"
    "v_cndmask_b32 v218, v190, v186, s[66:67]\n"
    "v_cndmask_b32 v217, v189, v185, s[64:65]\n"
    "v_mfma_f32_16x16x32_bf16 v[180:183], v[8:11], v[136:139], v[180:183]\n"
    "v_cndmask_b32 v223, v211, v191, s[68:69]\n"
    "v_mfma_f32_16x16x32_bf16 v[180:183], v[12:15], v[128:131], v[180:183]\n"
    "v_cndmask_b32 v222, v210, v190, s[66:67]\n"
    "v_cndmask_b32 v221, v209, v189, s[64:65]\n"
    "v_mfma_f32_16x16x32_bf16 v[180:183], v[16:19], v[120:123], v[180:183]\n"
    "v_add_f32_dpp v218, v219, v218 wave_ror:1 row_mask:0xf bank_mask:0xf\n"
    "v_mfma_f32_16x16x32_bf16 v[180:183], v[20:23], v[112:115], v[180:183]\n"
    "v_add_f32_dpp v222, v223, v222 wave_ror:1 row_mask:0xf bank_mask:0xf\n"
    "v_add_f32_dpp v217, v218, v217 wave_ror:1 row_mask:0xf bank_mask:0xf\n"
    "v_mfma_f32_16x16x32_bf16 v[180:183], v[24:27], v[104:107], v[180:183]\n"
    "v_add_f32_dpp v221, v222, v221 wave_ror:1 row_mask:0xf bank_mask:0xf\n"
    "v_mfma_f32_16x16x32_bf16 v[180:183], v[28:31], v[96:99], v[180:183]\n"
    "v_add_f32_dpp v216, v217, v188 wave_ror:1 row_mask:0xf bank_mask:0xf\n"
    "v_add_f32_dpp v220, v221, v208 wave_ror:1 row_mask:0xf bank_mask:0xf\n"
    "v_mfma_f32_16x16x32_bf16 v[180:183], v[32:35], v[88:91], v[180:183]\n"
    "s_waitcnt lgkmcnt(0)\n"
    "v_fmac_f32 v216, s71, v226\n"
    "v_mfma_f32_16x16x32_bf16 v[180:183], v[36:39], v[80:83], v[180:183]\n"
    "v_fmac_f32 v220, s71, v227\n"
    "v_cvt_pk_bf16_f32 v228, v216, v220\n"
    "v_mfma_f32_16x16x32_bf16 v[180:183], v[40:43], v[72:75], v[180:183]\n"
    "ds_write_b16 v229, v228 offset:12800\n"
    "v_mfma_f32_16x16x32_bf16 v[180:183], v[44:47], v[64:67], v[180:183]\n"
    "ds_write_b16_d16_hi v229, v228 offset:13312\n"
    "ds_read_u16_d16_hi v224, v229 offset:0\n"
    "v_mfma_f32_16x16x32_bf16 v[192:195], v[44:47], v[68:71], 0\n"
    "ds_read_u16_d16_hi v225, v229 offset:512\n"
    "v_mfma_f32_16x16x32_bf16 v[192:195], v[40:43], v[76:79], v[192:195]\n"
    "v_cndmask_b32 v219, v199, 0, s[68:69]\n"
    "v_cndmask_b32 v218, v198, 0, s[66:67]\n"
    "v_mfma_f32_16x16x32_bf16 v[192:195], v[36:39], v[84:87], v[192:195]\n"
    "v_cndmask_b32 v217, v197, 0, s[64:65]\n"
    "v_mfma_f32_16x16x32_bf16 v[192:195], v[32:35], v[92:95], v[192:195]\n"
    "v_cndmask_b32 v223, v179, v199, s[68:69]\n"
    "v_cndmask_b32 v222, v178, v198, s[66:67]\n"
    "v_mfma_f32_16x16x32_bf16 v[192:195], v[28:31], v[100:103], v[192:195]\n"
    "v_cndmask_b32 v221, v177, v197, s[64:65]\n"
    "v_mfma_f32_16x16x32_bf16 v[192:195], v[24:27], v[108:111], v[192:195]\n"
    "v_add_f32_dpp v218, v219, v218 wave_ror:1 row_mask:0xf bank_mask:0xf\n"
    "v_add_f32_dpp v222, v223, v222 wave_ror:1 row_mask:0xf bank_mask:0xf\n"
    "v_mfma_f32_16x16x32_bf16 v[192:195], v[20:23], v[116:119], v[192:195]\n"
    "v_add_f32_dpp v217, v218, v217 wave_ror:1 row_mask:0xf bank_mask:0xf\n"
    "v_mfma_f32_16x16x32_bf16 v[192:195], v[16:19], v[124:127], v[192:195]\n"
    "v_add_f32_dpp v221, v222, v221 wave_ror:1 row_mask:0xf bank_mask:0xf\n"
    "v_add_f32_dpp v216, v217, v196 wave_ror:1 row_mask:0xf bank_mask:0xf\n"
    "v_mfma_f32_16x16x32_bf16 v[192:195], v[12:15], v[132:135], v[192:195]\n"
    "v_add_f32_dpp v220, v221, v176 wave_ror:1 row_mask:0xf bank_mask:0xf\n"
    "v_mfma_f32_16x16x32_bf16 v[192:195], v[8:11], v[140:143], v[192:195]\n"
    "s_waitcnt lgkmcnt(0)\n"
    "v_fmac_f32 v216, s71, v224\n"
    "v_fmac_f32 v220, s71, v225\n"
    "v_mfma_f32_16x16x32_bf16 v[192:195], v[4:7], v[148:151], v[192:195]\n"
    "v_cvt_pk_bf16_f32 v228, v216, v220\n"
    "v_mfma_f32_16x16x32_bf16 v[192:195], v[0:3], v[156:159], v[192:195]\n"
    "ds_write_b16 v229, v228 offset:0\n"
    "ds_write_b16_d16_hi v229, v228 offset:512\n"
    "v_mfma_f32_16x16x32_bf16 v[204:207], v[0:3], v[72:75], 0\n"
    "v_mfma_f32_16x16x32_bf16 v[204:207], v[4:7], v[64:67], v[204:207]\n"
    "v_mfma_f32_16x16x32_bf16 v[168:171], v[4:7], v[68:71], 0\n"
    "v_mfma_f32_16x16x32_bf16 v[168:171], v[0:3], v[76:79], v[168:171]\n"
    "ds_read_u16_d16_hi v226, v229 offset:11776\n"
    "ds_read_u16_d16_hi v227, v229 offset:12288\n"
    "v_mfma_f32_16x16x32_bf16 v[172:175], v[0:3], v[144:147], 0\n"
    "v_cndmask_b32 v219, v195, v183, s[68:69]\n"
    "v_mfma_f32_16x16x32_bf16 v[172:175], v[4:7], v[136:139], v[172:175]\n"
    "v_cndmask_b32 v218, v194, v182, s[66:67]\n"
    "v_cndmask_b32 v217, v193, v181, s[64:65]\n"
    "v_mfma_f32_16x16x32_bf16 v[172:175], v[8:11], v[128:131], v[172:175]\n"
    "v_cndmask_b32 v223, v187, v195, s[68:69]\n"
    "v_mfma_f32_16x16x32_bf16 v[172:175], v[12:15], v[120:123], v[172:175]\n"
    "v_cndmask_b32 v222, v186, v194, s[66:67]\n"
    "v_cndmask_b32 v221, v185, v193, s[64:65]\n"
    "v_mfma_f32_16x16x32_bf16 v[172:175], v[16:19], v[112:115], v[172:175]\n"
    "v_add_f32_dpp v218, v219, v218 wave_ror:1 row_mask:0xf bank_mask:0xf\n"
    "v_mfma_f32_16x16x32_bf16 v[172:175], v[20:23], v[104:107], v[172:175]\n"
    "v_add_f32_dpp v222, v223, v222 wave_ror:1 row_mask:0xf bank_mask:0xf\n"
    "v_add_f32_dpp v217, v218, v217 wave_ror:1 row_mask:0xf bank_mask:0xf\n"
    "v_mfma_f32_16x16x32_bf16 v[172:175], v[24:27], v[96:99], v[172:175]\n"
    "v_add_f32_dpp v221, v222, v221 wave_ror:1 row_mask:0xf bank_mask:0xf\n"
    "v_mfma_f32_16x16x32_bf16 v[172:175], v[28:31], v[88:91], v[172:175]\n"
    "v_add_f32_dpp v216, v217, v192 wave_ror:1 row_mask:0xf bank_mask:0xf\n"
    "v_add_f32_dpp v220, v221, v184 wave_ror:1 row_mask:0xf bank_mask:0xf\n"
    "v_mfma_f32_16x16x32_bf16 v[172:175], v[32:35], v[80:83], v[172:175]\n"
    "s_waitcnt lgkmcnt(0)\n"
    "v_fmac_f32 v216, s71, v226\n"
    "v_mfma_f32_16x16x32_bf16 v[172:175], v[36:39], v[72:75], v[172:175]\n"
    "v_fmac_f32 v220, s71, v227\n"
    "v_cvt_pk_bf16_f32 v228, v216, v220\n"
    "v_mfma_f32_16x16x32_bf16 v[172:175], v[40:43], v[64:67], v[172:175]\n"
    "ds_write_b16 v229, v228 offset:11776\n"
    "v_mfma_f32_16x16x32_bf16 v[200:203], v[40:43], v[68:71], 0\n"
    "ds_write_b16_d16_hi v229, v228 offset:12288\n"
    "v_mov_b32 v248, 1\n"
    "ds_write_b32 v246, v248 offset:48\n"
    "ds_read_u16_d16_hi v224, v229 offset:1024\n"
    "v_mfma_f32_16x16x32_bf16 v[200:203], v[36:39], v[76:79], v[200:203]\n"
    "ds_read_u16_d16_hi v225, v229 offset:1536\n"
    "v_mfma_f32_16x16x32_bf16 v[200:203], v[32:35], v[84:87], v[200:203]\n"
    "v_cndmask_b32 v219, v207, v179, s[68:69]\n"
    "v_cndmask_b32 v218, v206, v178, s[66:67]\n"
    "v_mfma_f32_16x16x32_bf16 v[200:203], v[28:31], v[92:95], v[200:203]\n"
    "v_cndmask_b32 v217, v205, v177, s[64:65]\n"
    "v_mfma_f32_16x16x32_bf16 v[200:203], v[24:27], v[100:103], v[200:203]\n"
    "v_cndmask_b32 v223, v171, v207, s[68:69]\n"
    "v_cndmask_b32 v222, v170, v206, s[66:67]\n"
    "v_mfma_f32_16x16x32_bf16 v[200:203], v[20:23], v[108:111], v[200:203]\n"
    "v_cndmask_b32 v221, v169, v205, s[64:65]\n"
    "v_mfma_f32_16x16x32_bf16 v[200:203], v[16:19], v[116:119], v[200:203]\n"
    "v_add_f32_dpp v218, v219, v218 wave_ror:1 row_mask:0xf bank_mask:0xf\n"
    "v_add_f32_dpp v222, v223, v222 wave_ror:1 row_mask:0xf bank_mask:0xf\n"
    "v_mfma_f32_16x16x32_bf16 v[200:203], v[12:15], v[124:127], v[200:203]\n"
    "v_add_f32_dpp v217, v218, v217 wave_ror:1 row_mask:0xf bank_mask:0xf\n"
    "v_mfma_f32_16x16x32_bf16 v[200:203], v[8:11], v[132:135], v[200:203]\n"
    "v_add_f32_dpp v221, v222, v221 wave_ror:1 row_mask:0xf bank_mask:0xf\n"
    "v_add_f32_dpp v216, v217, v204 wave_ror:1 row_mask:0xf bank_mask:0xf\n"
    "v_mfma_f32_16x16x32_bf16 v[200:203], v[4:7], v[140:143], v[200:203]\n"
    "v_add_f32_dpp v220, v221, v168 wave_ror:1 row_mask:0xf bank_mask:0xf\n"
    "v_mfma_f32_16x16x32_bf16 v[200:203], v[0:3], v[148:151], v[200:203]\n"
    "s_waitcnt lgkmcnt(0)\n"
    "v_fmac_f32 v216, s71, v224\n"
    "v_fmac_f32 v220, s71, v225\n"
    "v_mfma_f32_16x16x32_bf16 v[212:215], v[0:3], v[80:83], 0\n"
    "v_cvt_pk_bf16_f32 v228, v216, v220\n"
    "v_mfma_f32_16x16x32_bf16 v[212:215], v[4:7], v[72:75], v[212:215]\n"
    "ds_write_b16 v229, v228 offset:1024\n"
    "ds_write_b16_d16_hi v229, v228 offset:1536\n"
    "v_mfma_f32_16x16x32_bf16 v[212:215], v[8:11], v[64:67], v[212:215]\n"
    "v_mfma_f32_16x16x32_bf16 v[160:163], v[8:11], v[68:71], 0\n"
    "ds_read_u16_d16_hi v226, v229 offset:10752\n"
    "ds_read_u16_d16_hi v227, v229 offset:11264\n"
    "v_mfma_f32_16x16x32_bf16 v[160:163], v[4:7], v[76:79], v[160:163]\n"
    "v_cndmask_b32 v219, v203, v175, s[68:69]\n"
    "v_mfma_f32_16x16x32_bf16 v[160:163], v[0:3], v[84:87], v[160:163]\n"
    "v_cndmask_b32 v218, v202, v174, s[66:67]\n"
    "v_cndmask_b32 v217, v201, v173, s[64:65]\n"
    "v_mfma_f32_16x16x32_bf16 v[164:167], v[0:3], v[136:139], 0\n"
    "v_cndmask_b32 v223, v183, v203, s[68:69]\n"
    "v_mfma_f32_16x16x32_bf16 v[164:167], v[4:7], v[128:131], v[164:167]\n"
    "v_cndmask_b32 v222, v182, v202, s[66:67]\n"
    "v_cndmask_b32 v221, v181, v201, s[64:65]\n"
    "v_mfma_f32_16x16x32_bf16 v[164:167], v[8:11], v[120:123], v[164:167]\n"
    "v_add_f32_dpp v218, v219, v218 wave_ror:1 row_mask:0xf bank_mask:0xf\n"
    "v_mfma_f32_16x16x32_bf16 v[164:167], v[12:15], v[112:115], v[164:167]\n"
    "v_add_f32_dpp v222, v223, v222 wave_ror:1 row_mask:0xf bank_mask:0xf\n"
    "v_add_f32_dpp v217, v218, v217 wave_ror:1 row_mask:0xf bank_mask:0xf\n"
    "v_mfma_f32_16x16x32_bf16 v[164:167], v[16:19], v[104:107], v[164:167]\n"
    "v_add_f32_dpp v221, v222, v221 wave_ror:1 row_mask:0xf bank_mask:0xf\n"
    "v_mfma_f32_16x16x32_bf16 v[164:167], v[20:23], v[96:99], v[164:167]\n"
    "v_add_f32_dpp v216, v217, v200 wave_ror:1 row_mask:0xf bank_mask:0xf\n"
    "v_add_f32_dpp v220, v221, v180 wave_ror:1 row_mask:0xf bank_mask:0xf\n"
    "v_mfma_f32_16x16x32_bf16 v[164:167], v[24:27], v[88:91], v[164:167]\n"
    "s_waitcnt lgkmcnt(0)\n"
    "v_fmac_f32 v216, s71, v226\n"
    "v_mfma_f32_16x16x32_bf16 v[164:167], v[28:31], v[80:83], v[164:167]\n"
    "v_fmac_f32 v220, s71, v227\n"
    "v_cvt_pk_bf16_f32 v228, v216, v220\n"
    "v_mfma_f32_16x16x32_bf16 v[164:167], v[32:35], v[72:75], v[164:167]\n"
    "ds_write_b16 v229, v228 offset:10752\n"
    "v_mfma_f32_16x16x32_bf16 v[164:167], v[36:39], v[64:67], v[164:167]\n"
    "ds_write_b16_d16_hi v229, v228 offset:11264\n"
    "ds_read_u16_d16_hi v224, v229 offset:2048\n"
    "v_mfma_f32_16x16x32_bf16 v[208:211], v[36:39], v[68:71], 0\n"
    "ds_read_u16_d16_hi v225, v229 offset:2560\n"
    "v_mfma_f32_16x16x32_bf16 v[208:211], v[32:35], v[76:79], v[208:211]\n"
    "v_cndmask_b32 v219, v215, v171, s[68:69]\n"
    "v_cndmask_b32 v218, v214, v170, s[66:67]\n"
    "v_mfma_f32_16x16x32_bf16 v[208:211], v[28:31], v[84:87], v[208:211]\n"
    "v_cndmask_b32 v217, v213, v169, s[64:65]\n"
    "v_mfma_f32_16x16x32_bf16 v[208:211], v[24:27], v[92:95], v[208:211]\n"
    "v_cndmask_b32 v223, v163, v215, s[68:69]\n"
    "v_cndmask_b32 v222, v162, v214, s[66:67]\n"
    "v_mfma_f32_16x16x32_bf16 v[208:211], v[20:23], v[100:103], v[208:211]\n"
    "v_cndmask_b32 v221, v161, v213, s[64:65]\n"
    "v_mfma_f32_16x16x32_bf16 v[208:211], v[16:19], v[108:111], v[208:211]\n"
    "v_add_f32_dpp v218, v219, v218 wave_ror:1 row_mask:0xf bank_mask:0xf\n"
    "v_add_f32_dpp v222, v223, v222 wave_ror:1 row_mask:0xf bank_mask:0xf\n"
    "v_mfma_f32_16x16x32_bf16 v[208:211], v[12:15], v[116:119], v[208:211]\n"
    "v_add_f32_dpp v217, v218, v217 wave_ror:1 row_mask:0xf bank_mask:0xf\n"
    "v_mfma_f32_16x16x32_bf16 v[208:211], v[8:11], v[124:127], v[208:211]\n"
    "v_add_f32_dpp v221, v222, v221 wave_ror:1 row_mask:0xf bank_mask:0xf\n"
    "v_add_f32_dpp v216, v217, v212 wave_ror:1 row_mask:0xf bank_mask:0xf\n"
    "v_mfma_f32_16x16x32_bf16 v[208:211], v[4:7], v[132:135], v[208:211]\n"
    "v_add_f32_dpp v220, v221, v160 wave_ror:1 row_mask:0xf bank_mask:0xf\n"
    "v_mfma_f32_16x16x32_bf16 v[208:211], v[0:3], v[140:143], v[208:211]\n"
    "s_waitcnt lgkmcnt(0)\n"
    "v_fmac_f32 v216, s71, v224\n"
    "v_fmac_f32 v220, s71, v225\n"
    "v_mfma_f32_16x16x32_bf16 v[188:191], v[0:3], v[88:91], 0\n"
    "v_cvt_pk_bf16_f32 v228, v216, v220\n"
    "v_mfma_f32_16x16x32_bf16 v[188:191], v[4:7], v[80:83], v[188:191]\n"
    "ds_write_b16 v229, v228 offset:2048\n"
    "ds_write_b16_d16_hi v229, v228 offset:2560\n"
    "v_mfma_f32_16x16x32_bf16 v[188:191], v[8:11], v[72:75], v[188:191]\n"
    "v_mfma_f32_16x16x32_bf16 v[188:191], v[12:15], v[64:67], v[188:191]\n"
    "ds_read_u16_d16_hi v226, v229 offset:9728\n"
    "ds_read_u16_d16_hi v227, v229 offset:10240\n"
    "v_mfma_f32_16x16x32_bf16 v[196:199], v[12:15], v[68:71], 0\n"
    "v_cndmask_b32 v219, v211, v167, s[68:69]\n"
    "v_mfma_f32_16x16x32_bf16 v[196:199], v[8:11], v[76:79], v[196:199]\n"
    "v_cndmask_b32 v218, v210, v166, s[66:67]\n"
    "v_cndmask_b32 v217, v209, v165, s[64:65]\n"
    "v_mfma_f32_16x16x32_bf16 v[196:199], v[4:7], v[84:87], v[196:199]\n"
    "v_cndmask_b32 v223, v175, v211, s[68:69]\n"
    "v_mfma_f32_16x16x32_bf16 v[196:199], v[0:3], v[92:95], v[196:199]\n"
    "v_cndmask_b32 v222, v174, v210, s[66:67]\n"
    "v_cndmask_b32 v221, v173, v209, s[64:65]\n"
    "v_mfma_f32_16x16x32_bf16 v[184:187], v[0:3], v[128:131], 0\n"
    "v_add_f32_dpp v218, v219, v218 wave_ror:1 row_mask:0xf bank_mask:0xf\n"
    "v_mfma_f32_16x16x32_bf16 v[184:187], v[4:7], v[120:123], v[184:187]\n"
    "v_add_f32_dpp v222, v223, v222 wave_ror:1 row_mask:0xf bank_mask:0xf\n"
    "v_add_f32_dpp v217, v218, v217 wave_ror:1 row_mask:0xf bank_mask:0xf\n"
    "v_mfma_f32_16x16x32_bf16 v[184:187], v[8:11], v[112:115], v[184:187]\n"
    "v_add_f32_dpp v221, v222, v221 wave_ror:1 row_mask:0xf bank_mask:0xf\n"
    "v_mfma_f32_16x16x32_bf16 v[184:187], v[12:15], v[104:107], v[184:187]\n"
    "v_add_f32_dpp v216, v217, v208 wave_ror:1 row_mask:0xf bank_mask:0xf\n"
    "v_add_f32_dpp v220, v221, v172 wave_ror:1 row_mask:0xf bank_mask:0xf\n"
    "v_mfma_f32_16x16x32_bf16 v[184:187], v[16:19], v[96:99], v[184:187]\n"
    "s_waitcnt lgkmcnt(0)\n"
    "v_fmac_f32 v216, s71, v226\n"
    "v_mfma_f32_16x16x32_bf16 v[184:187], v[20:23], v[88:91], v[184:187]\n"
    "v_fmac_f32 v220, s71, v227\n"
    "v_cvt_pk_bf16_f32 v228, v216, v220\n"
    "v_mfma_f32_16x16x32_bf16 v[184:187], v[24:27], v[80:83], v[184:187]\n"
    "ds_write_b16 v229, v228 offset:9728\n"
    "v_mfma_f32_16x16x32_bf16 v[184:187], v[28:31], v[72:75], v[184:187]\n"
    "ds_write_b16_d16_hi v229, v228 offset:10240\n"
    "ds_read_u16_d16_hi v224, v229 offset:3072\n"
    "v_mfma_f32_16x16x32_bf16 v[184:187], v[32:35], v[64:67], v[184:187]\n"
    "ds_read_u16_d16_hi v225, v229 offset:3584\n"
    "v_mfma_f32_16x16x32_bf16 v[192:195], v[32:35], v[68:71], 0\n"
    "v_cndmask_b32 v219, v191, v163, s[68:69]\n"
    "v_cndmask_b32 v218, v190, v162, s[66:67]\n"
    "v_mfma_f32_16x16x32_bf16 v[192:195], v[28:31], v[76:79], v[192:195]\n"
    "v_cndmask_b32 v217, v189, v161, s[64:65]\n"
    "v_mfma_f32_16x16x32_bf16 v[192:195], v[24:27], v[84:87], v[192:195]\n"
    "v_cndmask_b32 v223, v199, v191, s[68:69]\n"
    "v_cndmask_b32 v222, v198, v190, s[66:67]\n"
    "v_mfma_f32_16x16x32_bf16 v[192:195], v[20:23], v[92:95], v[192:195]\n"
    "v_cndmask_b32 v221, v197, v189, s[64:65]\n"
    "v_mfma_f32_16x16x32_bf16 v[192:195], v[16:19], v[100:103], v[192:195]\n"
    "v_add_f32_dpp v218, v219, v218 wave_ror:1 row_mask:0xf bank_mask:0xf\n"
    "v_add_f32_dpp v222, v223, v222 wave_ror:1 row_mask:0xf bank_mask:0xf\n"
    "v_mfma_f32_16x16x32_bf16 v[192:195], v[12:15], v[108:111], v[192:195]\n"
    "v_add_f32_dpp v217, v218, v217 wave_ror:1 row_mask:0xf bank_mask:0xf\n"
    "v_mfma_f32_16x16x32_bf16 v[192:195], v[8:11], v[116:119], v[192:195]\n"
    "v_add_f32_dpp v221, v222, v221 wave_ror:1 row_mask:0xf bank_mask:0xf\n"
    "v_add_f32_dpp v216, v217, v188 wave_ror:1 row_mask:0xf bank_mask:0xf\n"
    "v_mfma_f32_16x16x32_bf16 v[192:195], v[4:7], v[124:127], v[192:195]\n"
    "v_add_f32_dpp v220, v221, v196 wave_ror:1 row_mask:0xf bank_mask:0xf\n"
    "v_mfma_f32_16x16x32_bf16 v[192:195], v[0:3], v[132:135], v[192:195]\n"
    "s_waitcnt lgkmcnt(0)\n"
    "v_fmac_f32 v216, s71, v224\n"
    "v_fmac_f32 v220, s71, v225\n"
    "v_mfma_f32_16x16x32_bf16 v[176:179], v[0:3], v[120:123], 0\n"
    "v_cvt_pk_bf16_f32 v228, v216, v220\n"
    "v_mfma_f32_16x16x32_bf16 v[176:179], v[4:7], v[112:115], v[176:179]\n"
    "ds_write_b16 v229, v228 offset:3072\n"
    "ds_write_b16_d16_hi v229, v228 offset:3584\n"
    "v_mov_b32 v248, 2\n"
    "ds_write_b32 v246, v248 offset:48\n"
    "v_mfma_f32_16x16x32_bf16 v[176:179], v[8:11], v[104:107], v[176:179]\n"
    "v_mfma_f32_16x16x32_bf16 v[176:179], v[12:15], v[96:99], v[176:179]\n"
    "ds_read_u16_d16_hi v226, v229 offset:8704\n"
    "ds_read_u16_d16_hi v227, v229 offset:9216\n"
    "v_mfma_f32_16x16x32_bf16 v[176:179], v[16:19], v[88:91], v[176:179]\n"
    "v_cndmask_b32 v219, v195, v187, s[68:69]\n"
    "v_mfma_f32_16x16x32_bf16 v[176:179], v[20:23], v[80:83], v[176:179]\n"
    "v_cndmask_b32 v218, v194, v186, s[66:67]\n"
    "v_cndmask_b32 v217, v193, v185, s[64:65]\n"
    "v_mfma_f32_16x16x32_bf16 v[176:179], v[24:27], v[72:75], v[176:179]\n"
    "v_cndmask_b32 v223, v167, v195, s[68:69]\n"
    "v_mfma_f32_16x16x32_bf16 v[176:179], v[28:31], v[64:67], v[176:179]\n"
    "v_cndmask_b32 v222, v166, v194, s[66:67]\n"
    "v_cndmask_b32 v221, v165, v193, s[64:65]\n"
    "v_mfma_f32_16x16x32_bf16 v[204:207], v[28:31], v[68:71], 0\n"
    "v_add_f32_dpp v218, v219, v218 wave_ror:1 row_mask:0xf bank_mask:0xf\n"
    "v_mfma_f32_16x16x32_bf16 v[204:207], v[24:27], v[76:79], v[204:207]\n"
    "v_add_f32_dpp v222, v223, v222 wave_ror:1 row_mask:0xf bank_mask:0xf\n"
    "v_add_f32_dpp v217, v218, v217 wave_ror:1 row_mask:0xf bank_mask:0xf\n"
    "v_mfma_f32_16x16x32_bf16 v[204:207], v[20:23], v[84:87], v[204:207]\n"
    "v_add_f32_dpp v221, v222, v221 wave_ror:1 row_mask:0xf bank_mask:0xf\n"
    "v_mfma_f32_16x16x32_bf16 v[204:207], v[16:19], v[92:95], v[204:207]\n"
    "v_add_f32_dpp v216, v217, v192 wave_ror:1 row_mask:0xf bank_mask:0xf\n"
    "v_add_f32_dpp v220, v221, v164 wave_ror:1 row_mask:0xf bank_mask:0xf\n"
    "v_mfma_f32_16x16x32_bf16 v[204:207], v[12:15], v[100:103], v[204:207]\n"
    "s_waitcnt lgkmcnt(0)\n"
    "v_fmac_f32 v216, s71, v226\n"
    "v_mfma_f32_16x16x32_bf16 v[204:207], v[8:11], v[108:111], v[204:207]\n"
    "v_fmac_f32 v220, s71, v227\n"
    "v_cvt_pk_bf16_f32 v228, v216, v220\n"
    "v_mfma_f32_16x16x32_bf16 v[204:207], v[4:7], v[116:119], v[204:207]\n"
    "ds_write_b16 v229, v228 offset:8704\n"
    "v_mfma_f32_16x16x32_bf16 v[204:207], v[0:3], v[124:127], v[204:207]\n"
    "ds_write_b16_d16_hi v229, v228 offset:9216\n"
    "v_mfma_f32_16x16x32_bf16 v[180:183], v[0:3], v[96:99], 0\n"
    "v_mfma_f32_16x16x32_bf16 v[180:183], v[4:7], v[88:91], v[180:183]\n"
    "v_mfma_f32_16x16x32_bf16 v[180:183], v[8:11], v[80:83], v[180:183]\n"
    "v_mfma_f32_16x16x32_bf16 v[180:183], v[12:15], v[72:75], v[180:183]\n"
    "ds_read_u16_d16_hi v224, v229 offset:7680\n"
    "ds_read_u16_d16_hi v225, v229 offset:8192\n"
    "v_mfma_f32_16x16x32_bf16 v[180:183], v[16:19], v[64:67], v[180:183]\n"
    "v_cndmask_b32 v219, v207, v179, s[68:69]\n"
    "v_mfma_f32_16x16x32_bf16 v[200:203], v[16:19], v[68:71], 0\n"
    "v_cndmask_b32 v218, v206, v178, s[66:67]\n"
    "v_cndmask_b32 v217, v205, v177, s[64:65]\n"
    "v_mfma_f32_16x16x32_bf16 v[200:203], v[12:15], v[76:79], v[200:203]\n"
    "v_cndmask_b32 v223, v187, v207, s[68:69]\n"
    "v_mfma_f32_16x16x32_bf16 v[200:203], v[8:11], v[84:87], v[200:203]\n"
    "v_cndmask_b32 v222, v186, v206, s[66:67]\n"
    "v_cndmask_b32 v221, v185, v205, s[64:65]\n"
    "v_mfma_f32_16x16x32_bf16 v[200:203], v[4:7], v[92:95], v[200:203]\n"
    "v_add_f32_dpp v218, v219, v218 wave_ror:1 row_mask:0xf bank_mask:0xf\n"
    "v_mfma_f32_16x16x32_bf16 v[200:203], v[0:3], v[100:103], v[200:203]\n"
    "v_add_f32_dpp v222, v223, v222 wave_ror:1 row_mask:0xf bank_mask:0xf\n"
    "v_add_f32_dpp v217, v218, v217 wave_ror:1 row_mask:0xf bank_mask:0xf\n"
    "v_mfma_f32_16x16x32_bf16 v[168:171], v[0:3], v[104:107], 0\n"
    "v_add_f32_dpp v221, v222, v221 wave_ror:1 row_mask:0xf bank_mask:0xf\n"
    "v_mfma_f32_16x16x32_bf16 v[168:171], v[4:7], v[96:99], v[168:171]\n"
    "v_add_f32_dpp v216, v217, v204 wave_ror:1 row_mask:0xf bank_mask:0xf\n"
    "v_add_f32_dpp v220, v221, v184 wave_ror:1 row_mask:0xf bank_mask:0xf\n"
    "v_mfma_f32_16x16x32_bf16 v[168:171], v[8:11], v[88:91], v[168:171]\n"
    "s_waitcnt lgkmcnt(0)\n"
    "v_fmac_f32 v216, s71, v224\n"
    "v_mfma_f32_16x16x32_bf16 v[168:171], v[12:15], v[80:83], v[168:171]\n"
    "v_fmac_f32 v220, s71, v225\n"
    "v_cvt_pk_bf16_f32 v228, v216, v220\n"
    "v_mfma_f32_16x16x32_bf16 v[168:171], v[16:19], v[72:75], v[168:171]\n"
    "ds_write_b16 v229, v228 offset:7680\n"
    "v_mfma_f32_16x16x32_bf16 v[168:171], v[20:23], v[64:67], v[168:171]\n"
    "ds_write_b16_d16_hi v229, v228 offset:8192\n"
    "v_mov_b32 v248, 3\n"
    "ds_write_b32 v246, v248 offset:48\n"
    "ds_read_u16_d16_hi v226, v229 offset:4096\n"
    "v_mfma_f32_16x16x32_bf16 v[212:215], v[20:23], v[68:71], 0\n"
    "ds_read_b32 v247, v245\n"
    "ds_read_u16_d16_hi v227, v229 offset:4608\n"
    "v_mfma_f32_16x16x32_bf16 v[212:215], v[16:19], v[76:79], v[212:215]\n"
    "v_cndmask_b32 v219, v183, v199, s[68:69]\n"
    "v_cndmask_b32 v218, v182, v198, s[66:67]\n"
    "v_mfma_f32_16x16x32_bf16 v[212:215], v[12:15], v[84:87], v[212:215]\n"
    "v_cndmask_b32 v217, v181, v197, s[64:65]\n"
    "v_mfma_f32_16x16x32_bf16 v[212:215], v[8:11], v[92:95], v[212:215]\n"
    "v_cndmask_b32 v223, v203, v183, s[68:69]\n"
    "v_cndmask_b32 v222, v202, v182, s[66:67]\n"
    "v_mfma_f32_16x16x32_bf16 v[212:215], v[4:7], v[100:103], v[212:215]\n"
    "v_cndmask_b32 v221, v201, v181, s[64:65]\n"
    "v_mfma_f32_16x16x32_bf16 v[212:215], v[0:3], v[108:111], v[212:215]\n"
    "s_waitcnt lgkmcnt(0)\n"
    "v_cmp_gt_u32 vcc, s59, v247\n"
    "v_add_f32_dpp v218, v219, v218 wave_ror:1 row_mask:0xf bank_mask:0xf\n"
    "v_add_f32_dpp v222, v223, v222 wave_ror:1 row_mask:0xf bank_mask:0xf\n"
    "v_mfma_f32_16x16x32_bf16 v[172:175], v[0:3], v[112:115], 0\n"
    "s_cbranch_vccnz Lc14_%=\n"
    "Lb15_%=:\n"
    "v_add_f32_dpp v217, v218, v217 wave_ror:1 row_mask:0xf bank_mask:0xf\n"
    "v_mfma_f32_16x16x32_bf16 v[172:175], v[4:7], v[104:107], v[172:175]\n"
    "ds_read_b128 v[128:131], v230 offset:0\n"
    "v_add_f32_dpp v221, v222, v221 wave_ror:1 row_mask:0xf bank_mask:0xf\n"
    "v_mfma_f32_16x16x32_bf16 v[172:175], v[8:11], v[96:99], v[172:175]\n"
    "ds_read_b64_tr_b16 v[144:145], v231 offset:0\n"
    "v_add_f32_dpp v216, v217, v180 wave_ror:1 row_mask:0xf bank_mask:0xf\n"
    "v_mfma_f32_16x16x32_bf16 v[172:175], v[12:15], v[88:91], v[172:175]\n"
    "ds_read_b64_tr_b16 v[146:147], v231 offset:2048\n"
    "v_add_f32_dpp v220, v221, v200 wave_ror:1 row_mask:0xf bank_mask:0xf\n"
    "v_mfma_f32_16x16x32_bf16 v[172:175], v[16:19], v[80:83], v[172:175]\n"
    "ds_read_b128 v[132:135], v230 offset:1024\n"
    "v_fmac_f32 v216, s71, v226\n"
    "v_mfma_f32_16x16x32_bf16 v[172:175], v[20:23], v[72:75], v[172:175]\n"
    "ds_read_b128 v[136:139], v230 offset:2048\n"
    "v_fmac_f32 v220, s71, v227\n"
    "v_mfma_f32_16x16x32_bf16 v[172:175], v[24:27], v[64:67], v[172:175]\n"
    "ds_read_b128 v[140:143], v230 offset:3072\n"
    "v_cvt_pk_bf16_f32 v228, v216, v220\n"
    "v_mfma_f32_16x16x32_bf16 v[208:211], v[24:27], v[68:71], 0\n"
    "ds_read_b64_tr_b16 v[148:149], v232 offset:0\n"
    "ds_write_b16 v229, v228 offset:4096\n"
    "v_mfma_f32_16x16x32_bf16 v[208:211], v[20:23], v[76:79], v[208:211]\n"
    "ds_read_b64_tr_b16 v[150:151], v232 offset:2048\n"
    "ds_write_b16_d16_hi v229, v228 offset:4608\n"
    "v_mfma_f32_16x16x32_bf16 v[208:211], v[16:19], v[84:87], v[208:211]\n"
    "ds_read_b64_tr_b16 v[152:153], v231 offset:4096\n"
    "ds_read_u16_d16_hi v224, v229 offset:5120\n"
    "v_mfma_f32_16x16x32_bf16 v[208:211], v[12:15], v[92:95], v[208:211]\n"
    "ds_read_b64_tr_b16 v[154:155], v231 offset:6144\n"
    "ds_read_u16_d16_hi v225, v229 offset:5632\n"
    "v_mfma_f32_16x16x32_bf16 v[208:211], v[8:11], v[100:103], v[208:211]\n"
    "ds_read_b64_tr_b16 v[156:157], v232 offset:4096\n"
    "v_cndmask_b32 v219, v171, v203, s[68:69]\n"
    "v_mfma_f32_16x16x32_bf16 v[208:211], v[4:7], v[108:111], v[208:211]\n"
    "ds_read_b64_tr_b16 v[158:159], v232 offset:6144\n"
    "v_cndmask_b32 v218, v170, v202, s[66:67]\n"
    "v_mfma_f32_16x16x32_bf16 v[208:211], v[0:3], v[116:119], v[208:211]\n"
    "v_cndmask_b32 v217, v169, v201, s[64:65]\n"
    "v_cndmask_b32 v223, v215, v171, s[68:69]\n"
    "s_waitcnt lgkmcnt(6)\n"
    "v_mfma_f32_16x16x32_bf16 v[0:3], v[144:147], v[128:131], 0\n"
    "ds_read_b128 v[96:99], v230 offset:8192\n"
    "v_cndmask_b32 v222, v214, v170, s[66:67]\n"
    "v_mfma_f32_16x16x32_bf16 v[4:7], v[144:147], v[132:135], 0\n"
    "ds_read_b64_tr_b16 v[112:113], v231 offset:8192\n"
    "v_cndmask_b32 v221, v213, v169, s[64:65]\n"
    "v_mfma_f32_16x16x32_bf16 v[8:11], v[144:147], v[136:139], 0\n"
    "ds_read_b64_tr_b16 v[114:115], v231 offset:10240\n"
    "v_add_f32_dpp v218, v219, v218 wave_ror:1 row_mask:0xf bank_mask:0xf\n"
    "v_mfma_f32_16x16x32_bf16 v[12:15], v[144:147], v[140:143], 0\n"
    "ds_read_b128 v[100:103], v230 offset:9216\n"
    "v_add_f32_dpp v222, v223, v222 wave_ror:1 row_mask:0xf bank_mask:0xf\n"
    "v_mfma_f32_16x16x32_bf16 v[28:31], v[148:151], v[140:143], 0\n"
    "ds_read_b128 v[104:107], v230 offset:10240\n"
    "v_add_f32_dpp v217, v218, v217 wave_ror:1 row_mask:0xf bank_mask:0xf\n"
    "v_mfma_f32_16x16x32_bf16 v[24:27], v[148:151], v[136:139], 0\n"
    "ds_read_b128 v[108:111], v230 offset:11264\n"
    "v_add_f32_dpp v221, v222, v221 wave_ror:1 row_mask:0xf bank_mask:0xf\n"
    "v_mfma_f32_16x16x32_bf16 v[20:23], v[148:151], v[132:135], 0\n"
    "ds_read_b64_tr_b16 v[116:117], v232 offset:8192\n"
    "v_add_f32_dpp v216, v217, v168 wave_ror:1 row_mask:0xf bank_mask:0xf\n"
    "v_mfma_f32_16x16x32_bf16 v[16:19], v[148:151], v[128:131], 0\n"
    "ds_read_b64_tr_b16 v[118:119], v232 offset:10240\n"
    "v_add_f32_dpp v220, v221, v212 wave_ror:1 row_mask:0xf bank_mask:0xf\n"
    "s_waitcnt lgkmcnt(5)\n"
    "v_mfma_f32_16x16x32_bf16 v[32:35], v[152:155], v[128:131], 0\n"
    "ds_read_b64_tr_b16 v[120:121], v231 offset:12288\n"
    "v_fmac_f32 v216, s71, v224\n"
    "v_mfma_f32_16x16x32_bf16 v[36:39], v[152:155], v[132:135], 0\n"
    "ds_read_b64_tr_b16 v[122:123], v231 offset:14336\n"
    "v_fmac_f32 v220, s71, v225\n"
    "v_mfma_f32_16x16x32_bf16 v[40:43], v[152:155], v[136:139], 0\n"
    "ds_read_b64_tr_b16 v[124:125], v232 offset:12288\n"
    "v_cvt_pk_bf16_f32 v228, v216, v220\n"
    "v_mfma_f32_16x16x32_bf16 v[44:47], v[152:155], v[140:143], 0\n"
    "ds_read_b64_tr_b16 v[126:127], v232 offset:14336\n"
    "ds_write_b16 v229, v228 offset:5120\n"
    "v_mfma_f32_16x16x32_bf16 v[60:63], v[156:159], v[140:143], 0\n"
    "ds_write_b16_d16_hi v229, v228 offset:5632\n"
    "ds_read_u16_d16_hi v226, v229 offset:6144\n"
    "v_mfma_f32_16x16x32_bf16 v[56:59], v[156:159], v[136:139], 0\n"
    "ds_read_u16_d16_hi v227, v229 offset:6656\n"
    "v_cndmask_b32 v219, v175, v215, s[68:69]\n"
    "v_mfma_f32_16x16x32_bf16 v[52:55], v[156:159], v[132:135], 0\n"
    "v_cndmask_b32 v218, v174, v214, s[66:67]\n"
    "v_cndmask_b32 v217, v173, v213, s[64:65]\n"
    "v_mfma_f32_16x16x32_bf16 v[48:51], v[156:159], v[128:131], 0\n"
    "v_cndmask_b32 v223, v211, v175, s[68:69]\n"
    "v_cndmask_b32 v222, v210, v174, s[66:67]\n"
    "s_waitcnt lgkmcnt(0)\n"
    "s_barrier\n"
    "v_mfma_f32_16x16x32_bf16 v[0:3], v[112:115], v[96:99], v[0:3]\n"
    "ds_read_b128 v[64:67], v230 offset:16384\n"
    "v_cndmask_b32 v221, v209, v173, s[64:65]\n"
    "v_mfma_f32_16x16x32_bf16 v[4:7], v[112:115], v[100:103], v[4:7]\n"
    "ds_read_b128 v[68:71], v230 offset:24576\n"
    "v_add_f32_dpp v218, v219, v218 wave_ror:1 row_mask:0xf bank_mask:0xf\n"
    "v_mfma_f32_16x16x32_bf16 v[8:11], v[112:115], v[104:107], v[8:11]\n"
    "ds_read_b64_tr_b16 v[128:129], v231 offset:20480\n"
    "v_add_f32_dpp v222, v223, v222 wave_ror:1 row_mask:0xf bank_mask:0xf\n"
    "v_mfma_f32_16x16x32_bf16 v[12:15], v[112:115], v[108:111], v[12:15]\n"
    "ds_read_b64_tr_b16 v[130:131], v231 offset:22528\n"
    "v_add_f32_dpp v217, v218, v217 wave_ror:1 row_mask:0xf bank_mask:0xf\n"
    "v_mfma_f32_16x16x32_bf16 v[28:31], v[116:119], v[108:111], v[28:31]\n"
    "ds_read_b64_tr_b16 v[132:133], v231 offset:28672\n"
    "v_add_f32_dpp v221, v222, v221 wave_ror:1 row_mask:0xf bank_mask:0xf\n"
    "v_mfma_f32_16x16x32_bf16 v[24:27], v[116:119], v[104:107], v[24:27]\n"
    "ds_read_b64_tr_b16 v[134:135], v231 offset:30720\n"
    "v_add_f32_dpp v216, v217, v172 wave_ror:1 row_mask:0xf bank_mask:0xf\n"
    "v_mfma_f32_16x16x32_bf16 v[20:23], v[116:119], v[100:103], v[20:23]\n"
    "ds_read_b128 v[72:75], v230 offset:17408\n"
    "v_add_f32_dpp v220, v221, v208 wave_ror:1 row_mask:0xf bank_mask:0xf\n"
    "v_mfma_f32_16x16x32_bf16 v[16:19], v[116:119], v[96:99], v[16:19]\n"
    "ds_read_b128 v[76:79], v230 offset:25600\n"
    "v_fmac_f32 v216, s71, v226\n"
    "v_mfma_f32_16x16x32_bf16 v[32:35], v[120:123], v[96:99], v[32:35]\n"
    "ds_read_b128 v[80:83], v230 offset:18432\n"
    "v_fmac_f32 v220, s71, v227\n"
    "v_mfma_f32_16x16x32_bf16 v[36:39], v[120:123], v[100:103], v[36:39]\n"
    "ds_read_b128 v[84:87], v230 offset:26624\n"
    "v_cvt_pk_bf16_f32 v228, v216, v220\n"
    "v_mfma_f32_16x16x32_bf16 v[40:43], v[120:123], v[104:107], v[40:43]\n"
    "ds_read_b128 v[88:91], v230 offset:19456\n"
    "ds_write_b16 v229, v228 offset:6144\n"
    "v_mfma_f32_16x16x32_bf16 v[44:47], v[120:123], v[108:111], v[44:47]\n"
    "ds_read_b128 v[92:95], v230 offset:27648\n"
    "ds_write_b16_d16_hi v229, v228 offset:6656\n"
    "v_mfma_f32_16x16x32_bf16 v[60:63], v[124:127], v[108:111], v[60:63]\n"
    "ds_read_b64_tr_b16 v[136:137], v232 offset:20480\n"
    "ds_read_u16_d16_hi v224, v229 offset:7168\n"
    "v_mfma_f32_16x16x32_bf16 v[56:59], v[124:127], v[104:107], v[56:59]\n"
    "ds_read_b64_tr_b16 v[138:139], v232 offset:22528\n"
    "v_cndmask_b32 v219, v179, v211, s[68:69]\n"
    "v_mfma_f32_16x16x32_bf16 v[52:55], v[124:127], v[100:103], v[52:55]\n"
    "ds_read_b64_tr_b16 v[140:141], v232 offset:28672\n"
    "v_cndmask_b32 v218, v178, v210, s[66:67]\n"
    "v_mfma_f32_16x16x32_bf16 v[48:51], v[124:127], v[96:99], v[48:51]\n"
    "ds_read_b64_tr_b16 v[142:143], v232 offset:30720\n"
    "v_cndmask_b32 v217, v177, v209, s[64:65]\n"
    "v_add_f32_dpp v218, v219, v218 wave_ror:1 row_mask:0xf bank_mask:0xf\n"
    "s_nop 1\n"
    "v_add_f32_dpp v217, v218, v217 wave_ror:1 row_mask:0xf bank_mask:0xf\n"
    "s_nop 1\n"
    "v_add_f32_dpp v216, v217, v176 wave_ror:1 row_mask:0xf bank_mask:0xf\n"
    "s_waitcnt lgkmcnt(3)\n"
    "v_fmac_f32 v216, s71, v224\n"
    "v_cvt_pk_bf16_f32 v228, v216, v220\n"
    "ds_write_b16 v229, v228 offset:7168\n"
    "Lg2_%=:\n"
    "s_waitcnt lgkmcnt(5)\n"
    "v_mfma_f32_16x16x32_bf16 v[32:35], v[128:131], v[64:67], v[32:35]\n"
    "ds_read_b64_tr_b16 v[144:145], v231 offset:16384\n"
    "v_mfma_f32_16x16x32_bf16 v[32:35], v[132:135], v[68:71], v[32:35]\n"
    "ds_read_b64_tr_b16 v[146:147], v231 offset:18432\n"
    "v_mfma_f32_16x16x32_bf16 v[36:39], v[132:135], v[76:79], v[36:39]\n"
    "ds_read_b64_tr_b16 v[148:149], v231 offset:24576\n"
    "v_mfma_f32_16x16x32_bf16 v[36:39], v[128:131], v[72:75], v[36:39]\n"
    "ds_read_b64_tr_b16 v[150:151], v231 offset:26624\n"
    "v_mfma_f32_16x16x32_bf16 v[40:43], v[128:131], v[80:83], v[40:43]\n"
    "ds_read_b64_tr_b16 v[152:153], v232 offset:16384\n"
    "v_mfma_f32_16x16x32_bf16 v[40:43], v[132:135], v[84:87], v[40:43]\n"
    "ds_read_b64_tr_b16 v[154:155], v232 offset:18432\n"
    "s_waitcnt lgkmcnt(5)\n"
    "v_mfma_f32_16x16x32_bf16 v[44:47], v[132:135], v[92:95], v[44:47]\n"
    "ds_read_b64_tr_b16 v[156:157], v232 offset:24576\n"
    "v_mfma_f32_16x16x32_bf16 v[44:47], v[128:131], v[88:91], v[44:47]\n"
    "ds_read_b64_tr_b16 v[158:159], v232 offset:26624\n"
    "v_mfma_f32_16x16x32_bf16 v[60:63], v[136:139], v[88:91], v[60:63]\n"
    "v_mfma_f32_16x16x32_bf16 v[60:63], v[140:143], v[92:95], v[60:63]\n"
    "v_mfma_f32_16x16x32_bf16 v[56:59], v[140:143], v[84:87], v[56:59]\n"
    "v_mfma_f32_16x16x32_bf16 v[56:59], v[136:139], v[80:83], v[56:59]\n"
    "v_mfma_f32_16x16x32_bf16 v[52:55], v[136:139], v[72:75], v[52:55]\n"
    "v_mfma_f32_16x16x32_bf16 v[52:55], v[140:143], v[76:79], v[52:55]\n"
    "v_mfma_f32_16x16x32_bf16 v[48:51], v[140:143], v[68:71], v[48:51]\n"
    "v_mfma_f32_16x16x32_bf16 v[48:51], v[136:139], v[64:67], v[48:51]\n"
    "s_waitcnt lgkmcnt(0)\n"
    "s_barrier\n"
    "v_mfma_f32_16x16x32_bf16 v[0:3], v[144:147], v[64:67], v[0:3]\n"
    "ds_read_b128 v[96:99], v230 offset:32768\n"
    "v_mfma_f32_16x16x32_bf16 v[0:3], v[148:151], v[68:71], v[0:3]\n"
    "ds_read_b128 v[100:103], v230 offset:40960\n"
    "v_mfma_f32_16x16x32_bf16 v[4:7], v[148:151], v[76:79], v[4:7]\n"
    "ds_read_b64_tr_b16 v[128:129], v231 offset:36864\n"
    "v_mfma_f32_16x16x32_bf16 v[4:7], v[144:147], v[72:75], v[4:7]\n"
    "ds_read_b64_tr_b16 v[130:131], v231 offset:38912\n"
    "v_mfma_f32_16x16x32_bf16 v[8:11], v[144:147], v[80:83], v[8:11]\n"
    "ds_read_b64_tr_b16 v[132:133], v231 offset:45056\n"
    "v_mfma_f32_16x16x32_bf16 v[8:11], v[148:151], v[84:87], v[8:11]\n"
    "ds_read_b64_tr_b16 v[134:135], v231 offset:47104\n"
    "v_mfma_f32_16x16x32_bf16 v[12:15], v[148:151], v[92:95], v[12:15]\n"
    "ds_read_b128 v[104:107], v230 offset:33792\n"
    "v_mfma_f32_16x16x32_bf16 v[12:15], v[144:147], v[88:91], v[12:15]\n"
    "ds_read_b128 v[108:111], v230 offset:41984\n"
    "v_mfma_f32_16x16x32_bf16 v[28:31], v[152:155], v[88:91], v[28:31]\n"
    "ds_read_b128 v[112:115], v230 offset:34816\n"
    "v_mfma_f32_16x16x32_bf16 v[28:31], v[156:159], v[92:95], v[28:31]\n"
    "ds_read_b128 v[116:119], v230 offset:43008\n"
    "v_mfma_f32_16x16x32_bf16 v[24:27], v[156:159], v[84:87], v[24:27]\n"
    "ds_read_b128 v[120:123], v230 offset:35840\n"
    "v_mfma_f32_16x16x32_bf16 v[24:27], v[152:155], v[80:83], v[24:27]\n"
    "ds_read_b128 v[124:127], v230 offset:44032\n"
    "v_mfma_f32_16x16x32_bf16 v[20:23], v[152:155], v[72:75], v[20:23]\n"
    "ds_read_b64_tr_b16 v[136:137], v232 offset:36864\n"
    "v_mfma_f32_16x16x32_bf16 v[20:23], v[156:159], v[76:79], v[20:23]\n"
    "ds_read_b64_tr_b16 v[138:139], v232 offset:38912\n"
    "v_mfma_f32_16x16x32_bf16 v[16:19], v[156:159], v[68:71], v[16:19]\n"
    "ds_read_b64_tr_b16 v[140:141], v232 offset:45056\n"
    "v_mfma_f32_16x16x32_bf16 v[16:19], v[152:155], v[64:67], v[16:19]\n"
    "ds_read_b64_tr_b16 v[142:143], v232 offset:47104\n"
    "s_waitcnt lgkmcnt(5)\n"
    "v_mfma_f32_16x16x32_bf16 v[32:35], v[128:131], v[96:99], v[32:35]\n"
    "ds_read_b64_tr_b16 v[144:145], v231 offset:32768\n"
    "v_mfma_f32_16x16x32_bf16 v[32:35], v[132:135], v[100:103], v[32:35]\n"
    "ds_read_b64_tr_b16 v[146:147], v231 offset:34816\n"
    "v_mfma_f32_16x16x32_bf16 v[36:39], v[132:135], v[108:111], v[36:39]\n"
    "ds_read_b64_tr_b16 v[148:149], v231 offset:40960\n"
    "v_mfma_f32_16x16x32_bf16 v[36:39], v[128:131], v[104:107], v[36:39]\n"
    "ds_read_b64_tr_b16 v[150:151], v231 offset:43008\n"
    "v_mfma_f32_16x16x32_bf16 v[40:43], v[128:131], v[112:115], v[40:43]\n"
    "ds_read_b64_tr_b16 v[152:153], v232 offset:32768\n"
    "v_mfma_f32_16x16x32_bf16 v[40:43], v[132:135], v[116:119], v[40:43]\n"
    "ds_read_b64_tr_b16 v[154:155], v232 offset:34816\n"
    "s_waitcnt lgkmcnt(5)\n"
    "v_mfma_f32_16x16x32_bf16 v[44:47], v[132:135], v[124:127], v[44:47]\n"
    "ds_read_b64_tr_b16 v[156:157], v232 offset:40960\n"
    "v_mfma_f32_16x16x32_bf16 v[44:47], v[128:131], v[120:123], v[44:47]\n"
    "ds_read_b64_tr_b16 v[158:159], v232 offset:43008\n"
    "v_mfma_f32_16x16x32_bf16 v[60:63], v[136:139], v[120:123], v[60:63]\n"
    "v_mfma_f32_16x16x32_bf16 v[60:63], v[140:143], v[124:127], v[60:63]\n"
    "v_mfma_f32_16x16x32_bf16 v[56:59], v[140:143], v[116:119], v[56:59]\n"
    "v_mfma_f32_16x16x32_bf16 v[56:59], v[136:139], v[112:115], v[56:59]\n"
    "v_mfma_f32_16x16x32_bf16 v[52:55], v[136:139], v[104:107], v[52:55]\n"
    "v_mfma_f32_16x16x32_bf16 v[52:55], v[140:143], v[108:111], v[52:55]\n"
    "v_mfma_f32_16x16x32_bf16 v[48:51], v[140:143], v[100:103], v[48:51]\n"
    "v_mfma_f32_16x16x32_bf16 v[48:51], v[136:139], v[96:99], v[48:51]\n"
    "s_waitcnt lgkmcnt(0)\n"
    "s_barrier\n"
    "v_mfma_f32_16x16x32_bf16 v[0:3], v[144:147], v[96:99], v[0:3]\n"
    "ds_read_b128 v[64:67], v230 offset:49152\n"
    "v_mfma_f32_16x16x32_bf16 v[0:3], v[148:151], v[100:103], v[0:3]\n"
    "ds_read_b128 v[68:71], v230 offset:57344\n"
    "v_mfma_f32_16x16x32_bf16 v[4:7], v[148:151], v[108:111], v[4:7]\n"
    "ds_read_b64_tr_b16 v[128:129], v231 offset:53248\n"
    "v_mfma_f32_16x16x32_bf16 v[4:7], v[144:147], v[104:107], v[4:7]\n"
    "ds_read_b64_tr_b16 v[130:131], v231 offset:55296\n"
    "v_mfma_f32_16x16x32_bf16 v[8:11], v[144:147], v[112:115], v[8:11]\n"
    "ds_read_b64_tr_b16 v[132:133], v231 offset:61440\n"
    "v_mfma_f32_16x16x32_bf16 v[8:11], v[148:151], v[116:119], v[8:11]\n"
    "ds_read_b64_tr_b16 v[134:135], v231 offset:63488\n"
    "v_mfma_f32_16x16x32_bf16 v[12:15], v[148:151], v[124:127], v[12:15]\n"
    "ds_read_b128 v[72:75], v230 offset:50176\n"
    "v_mfma_f32_16x16x32_bf16 v[12:15], v[144:147], v[120:123], v[12:15]\n"
    "ds_read_b128 v[76:79], v230 offset:58368\n"
    "v_mfma_f32_16x16x32_bf16 v[28:31], v[152:155], v[120:123], v[28:31]\n"
    "ds_read_b128 v[80:83], v230 offset:51200\n"
    "v_mfma_f32_16x16x32_bf16 v[28:31], v[156:159], v[124:127], v[28:31]\n"
    "ds_read_b128 v[84:87], v230 offset:59392\n"
    "v_mfma_f32_16x16x32_bf16 v[24:27], v[156:159], v[116:119], v[24:27]\n"
    "ds_read_b128 v[88:91], v230 offset:52224\n"
    "v_mfma_f32_16x16x32_bf16 v[24:27], v[152:155], v[112:115], v[24:27]\n"
    "ds_read_b128 v[92:95], v230 offset:60416\n"
    "v_mfma_f32_16x16x32_bf16 v[20:23], v[152:155], v[104:107], v[20:23]\n"
    "ds_read_b64_tr_b16 v[136:137], v232 offset:53248\n"
    "v_mfma_f32_16x16x32_bf16 v[20:23], v[156:159], v[108:111], v[20:23]\n"
    "ds_read_b64_tr_b16 v[138:139], v232 offset:55296\n"
    "v_mfma_f32_16x16x32_bf16 v[16:19], v[156:159], v[100:103], v[16:19]\n"
    "ds_read_b64_tr_b16 v[140:141], v232 offset:61440\n"
    "v_mfma_f32_16x16x32_bf16 v[16:19], v[152:155], v[96:99], v[16:19]\n"
    "ds_read_b64_tr_b16 v[142:143], v232 offset:63488\n"
    "s_waitcnt lgkmcnt(5)\n"
    "v_mfma_f32_16x16x32_bf16 v[32:35], v[128:131], v[64:67], v[32:35]\n"
    "ds_read_b64_tr_b16 v[144:145], v231 offset:49152\n"
    "v_mfma_f32_16x16x32_bf16 v[32:35], v[132:135], v[68:71], v[32:35]\n"
    "ds_read_b64_tr_b16 v[146:147], v231 offset:51200\n"
    "v_mfma_f32_16x16x32_bf16 v[36:39], v[132:135], v[76:79], v[36:39]\n"
    "ds_read_b64_tr_b16 v[148:149], v231 offset:57344\n"
    "v_mfma_f32_16x16x32_bf16 v[36:39], v[128:131], v[72:75], v[36:39]\n"
    "ds_read_b64_tr_b16 v[150:151], v231 offset:59392\n"
    "v_mfma_f32_16x16x32_bf16 v[40:43], v[128:131], v[80:83], v[40:43]\n"
    "ds_read_b64_tr_b16 v[152:153], v232 offset:49152\n"
    "v_mfma_f32_16x16x32_bf16 v[40:43], v[132:135], v[84:87], v[40:43]\n"
    "ds_read_b64_tr_b16 v[154:155], v232 offset:51200\n"
    "s_waitcnt lgkmcnt(5)\n"
    "v_mfma_f32_16x16x32_bf16 v[44:47], v[132:135], v[92:95], v[44:47]\n"
    "ds_read_b64_tr_b16 v[156:157], v232 offset:57344\n"
    "v_mfma_f32_16x16x32_bf16 v[44:47], v[128:131], v[88:91], v[44:47]\n"
    "ds_read_b64_tr_b16 v[158:159], v232 offset:59392\n"
    "v_mfma_f32_16x16x32_bf16 v[60:63], v[136:139], v[88:91], v[60:63]\n"
    "v_mfma_f32_16x16x32_bf16 v[60:63], v[140:143], v[92:95], v[60:63]\n"
    "v_mfma_f32_16x16x32_bf16 v[56:59], v[140:143], v[84:87], v[56:59]\n"
    "v_mfma_f32_16x16x32_bf16 v[56:59], v[136:139], v[80:83], v[56:59]\n"
    "v_mfma_f32_16x16x32_bf16 v[52:55], v[136:139], v[72:75], v[52:55]\n"
    "v_mfma_f32_16x16x32_bf16 v[52:55], v[140:143], v[76:79], v[52:55]\n"
    "v_mfma_f32_16x16x32_bf16 v[48:51], v[140:143], v[68:71], v[48:51]\n"
    "v_mfma_f32_16x16x32_bf16 v[48:51], v[136:139], v[64:67], v[48:51]\n"
    "s_waitcnt lgkmcnt(0)\n"
    "s_barrier\n"
    "v_mfma_f32_16x16x32_bf16 v[0:3], v[144:147], v[64:67], v[0:3]\n"
    "ds_read_b128 v[96:99], v230 offset:0\n"
    "v_mfma_f32_16x16x32_bf16 v[0:3], v[148:151], v[68:71], v[0:3]\n"
    "ds_read_b128 v[100:103], v230 offset:8192\n"
    "v_mfma_f32_16x16x32_bf16 v[4:7], v[148:151], v[76:79], v[4:7]\n"
    "ds_read_b64_tr_b16 v[128:129], v231 offset:4096\n"
    "v_mfma_f32_16x16x32_bf16 v[4:7], v[144:147], v[72:75], v[4:7]\n"
    "ds_read_b64_tr_b16 v[130:131], v231 offset:6144\n"
    "v_mfma_f32_16x16x32_bf16 v[8:11], v[144:147], v[80:83], v[8:11]\n"
    "ds_read_b64_tr_b16 v[132:133], v231 offset:12288\n"
    "v_mfma_f32_16x16x32_bf16 v[8:11], v[148:151], v[84:87], v[8:11]\n"
    "ds_read_b64_tr_b16 v[134:135], v231 offset:14336\n"
    "v_mfma_f32_16x16x32_bf16 v[12:15], v[148:151], v[92:95], v[12:15]\n"
    "ds_read_b128 v[104:107], v230 offset:1024\n"
    "v_mfma_f32_16x16x32_bf16 v[12:15], v[144:147], v[88:91], v[12:15]\n"
    "ds_read_b128 v[108:111], v230 offset:9216\n"
    "v_mfma_f32_16x16x32_bf16 v[28:31], v[152:155], v[88:91], v[28:31]\n"
    "ds_read_b128 v[112:115], v230 offset:2048\n"
    "v_mfma_f32_16x16x32_bf16 v[28:31], v[156:159], v[92:95], v[28:31]\n"
    "ds_read_b128 v[116:119], v230 offset:10240\n"
    "v_mfma_f32_16x16x32_bf16 v[24:27], v[156:159], v[84:87], v[24:27]\n"
    "ds_read_b128 v[120:123], v230 offset:3072\n"
    "v_mfma_f32_16x16x32_bf16 v[24:27], v[152:155], v[80:83], v[24:27]\n"
    "ds_read_b128 v[124:127], v230 offset:11264\n"
    "v_mfma_f32_16x16x32_bf16 v[20:23], v[152:155], v[72:75], v[20:23]\n"
    "ds_read_b64_tr_b16 v[136:137], v232 offset:4096\n"
    "v_mfma_f32_16x16x32_bf16 v[20:23], v[156:159], v[76:79], v[20:23]\n"
    "ds_read_b64_tr_b16 v[138:139], v232 offset:6144\n"
    "v_mfma_f32_16x16x32_bf16 v[16:19], v[156:159], v[68:71], v[16:19]\n"
    "ds_read_b64_tr_b16 v[140:141], v232 offset:12288\n"
    "v_mfma_f32_16x16x32_bf16 v[16:19], v[152:155], v[64:67], v[16:19]\n"
    "ds_read_b64_tr_b16 v[142:143], v232 offset:14336\n"
    "s_waitcnt lgkmcnt(5)\n"
    "v_mfma_f32_16x16x32_bf16 v[32:35], v[128:131], v[96:99], v[32:35]\n"
    "ds_read_b64_tr_b16 v[144:145], v231 offset:0\n"
    "v_mfma_f32_16x16x32_bf16 v[32:35], v[132:135], v[100:103], v[32:35]\n"
    "ds_read_b64_tr_b16 v[146:147], v231 offset:2048\n"
    "v_mfma_f32_16x16x32_bf16 v[36:39], v[132:135], v[108:111], v[36:39]\n"
    "ds_read_b64_tr_b16 v[148:149], v231 offset:8192\n"
    "v_mfma_f32_16x16x32_bf16 v[36:39], v[128:131], v[104:107], v[36:39]\n"
    "ds_read_b64_tr_b16 v[150:151], v231 offset:10240\n"
    "v_mfma_f32_16x16x32_bf16 v[40:43], v[128:131], v[112:115], v[40:43]\n"
    "ds_read_b64_tr_b16 v[152:153], v232 offset:0\n"
    "v_mfma_f32_16x16x32_bf16 v[40:43], v[132:135], v[116:119], v[40:43]\n"
    "ds_read_b64_tr_b16 v[154:155], v232 offset:2048\n"
    "s_waitcnt lgkmcnt(5)\n"
    "v_mfma_f32_16x16x32_bf16 v[44:47], v[132:135], v[124:127], v[44:47]\n"
    "ds_read_b64_tr_b16 v[156:157], v232 offset:8192\n"
    "v_mfma_f32_16x16x32_bf16 v[44:47], v[128:131], v[120:123], v[44:47]\n"
    "ds_read_b64_tr_b16 v[158:159], v232 offset:10240\n"
    "v_mfma_f32_16x16x32_bf16 v[60:63], v[136:139], v[120:123], v[60:63]\n"
    "v_mfma_f32_16x16x32_bf16 v[60:63], v[140:143], v[124:127], v[60:63]\n"
    "v_mfma_f32_16x16x32_bf16 v[56:59], v[140:143], v[116:119], v[56:59]\n"
    "v_mfma_f32_16x16x32_bf16 v[56:59], v[136:139], v[112:115], v[56:59]\n"
    "v_mfma_f32_16x16x32_bf16 v[52:55], v[136:139], v[104:107], v[52:55]\n"
    "v_mfma_f32_16x16x32_bf16 v[52:55], v[140:143], v[108:111], v[52:55]\n"
    "v_mfma_f32_16x16x32_bf16 v[48:51], v[140:143], v[100:103], v[48:51]\n"
    "v_mfma_f32_16x16x32_bf16 v[48:51], v[136:139], v[96:99], v[48:51]\n"
    "s_waitcnt lgkmcnt(0)\n"
    "s_barrier\n"
    "v_mfma_f32_16x16x32_bf16 v[0:3], v[144:147], v[96:99], v[0:3]\n"
    "ds_read_b128 v[64:67], v230 offset:16384\n"
    "v_mfma_f32_16x16x32_bf16 v[0:3], v[148:151], v[100:103], v[0:3]\n"
    "ds_read_b128 v[68:71], v230 offset:24576\n"
    "v_mfma_f32_16x16x32_bf16 v[4:7], v[148:151], v[108:111], v[4:7]\n"
    "ds_read_b64_tr_b16 v[128:129], v231 offset:20480\n"
    "v_mfma_f32_16x16x32_bf16 v[4:7], v[144:147], v[104:107], v[4:7]\n"
    "ds_read_b64_tr_b16 v[130:131], v231 offset:22528\n"
    "v_mfma_f32_16x16x32_bf16 v[8:11], v[144:147], v[112:115], v[8:11]\n"
    "ds_read_b64_tr_b16 v[132:133], v231 offset:28672\n"
    "v_mfma_f32_16x16x32_bf16 v[8:11], v[148:151], v[116:119], v[8:11]\n"
    "ds_read_b64_tr_b16 v[134:135], v231 offset:30720\n"
    "v_mfma_f32_16x16x32_bf16 v[12:15], v[148:151], v[124:127], v[12:15]\n"
    "ds_read_b128 v[72:75], v230 offset:17408\n"
    "v_mfma_f32_16x16x32_bf16 v[12:15], v[144:147], v[120:123], v[12:15]\n"
    "ds_read_b128 v[76:79], v230 offset:25600\n"
    "v_mfma_f32_16x16x32_bf16 v[28:31], v[152:155], v[120:123], v[28:31]\n"
    "ds_read_b128 v[80:83], v230 offset:18432\n"
    "v_mfma_f32_16x16x32_bf16 v[28:31], v[156:159], v[124:127], v[28:31]\n"
    "ds_read_b128 v[84:87], v230 offset:26624\n"
    "v_mfma_f32_16x16x32_bf16 v[24:27], v[156:159], v[116:119], v[24:27]\n"
    "ds_read_b128 v[88:91], v230 offset:19456\n"
    "v_mfma_f32_16x16x32_bf16 v[24:27], v[152:155], v[112:115], v[24:27]\n"
    "ds_read_b128 v[92:95], v230 offset:27648\n"
    "v_mfma_f32_16x16x32_bf16 v[20:23], v[152:155], v[104:107], v[20:23]\n"
    "ds_read_b64_tr_b16 v[136:137], v232 offset:20480\n"
    "v_mfma_f32_16x16x32_bf16 v[20:23], v[156:159], v[108:111], v[20:23]\n"
    "ds_read_b64_tr_b16 v[138:139], v232 offset:22528\n"
    "v_mfma_f32_16x16x32_bf16 v[16:19], v[156:159], v[100:103], v[16:19]\n"
    "ds_read_b64_tr_b16 v[140:141], v232 offset:28672\n"
    "s_sub_u32 s58, s58, 1\n"
    "v_mfma_f32_16x16x32_bf16 v[16:19], v[152:155], v[96:99], v[16:19]\n"
    "s_cmp_lg_u32 s58, 1\n"
    "ds_read_b64_tr_b16 v[142:143], v232 offset:30720\n"
    "s_cbranch_scc1 Lg2_%=\n"
    "s_waitcnt lgkmcnt(4)\n"
    "v_mfma_f32_16x16x32_bf16 v[32:35], v[128:131], v[64:67], v[32:35]\n"
    "ds_read_b64_tr_b16 v[144:145], v231 offset:16384\n"
    "v_mfma_f32_16x16x32_bf16 v[32:35], v[132:135], v[68:71], v[32:35]\n"
    "ds_read_b64_tr_b16 v[146:147], v231 offset:18432\n"
    "v_mfma_f32_16x16x32_bf16 v[36:39], v[132:135], v[76:79], v[36:39]\n"
    "ds_read_b64_tr_b16 v[148:149], v231 offset:24576\n"
    "v_mfma_f32_16x16x32_bf16 v[36:39], v[128:131], v[72:75], v[36:39]\n"
    "ds_read_b64_tr_b16 v[150:151], v231 offset:26624\n"
    "v_mfma_f32_16x16x32_bf16 v[40:43], v[128:131], v[80:83], v[40:43]\n"
    "ds_read_b64_tr_b16 v[152:153], v232 offset:16384\n"
    "v_mfma_f32_16x16x32_bf16 v[40:43], v[132:135], v[84:87], v[40:43]\n"
    "ds_read_b64_tr_b16 v[154:155], v232 offset:18432\n"
    "v_mfma_f32_16x16x32_bf16 v[44:47], v[132:135], v[92:95], v[44:47]\n"
    "ds_read_b64_tr_b16 v[156:157], v232 offset:24576\n"
    "v_mfma_f32_16x16x32_bf16 v[44:47], v[128:131], v[88:91], v[44:47]\n"
    "ds_read_b64_tr_b16 v[158:159], v232 offset:26624\n"
    "s_barrier\n"
    "s_waitcnt lgkmcnt(4)\n"
    "v_mfma_f32_16x16x32_bf16 v[60:63], v[136:139], v[88:91], v[60:63]\n"
    "ds_read_b128 v[96:99], v230 offset:32768\n"
    "v_mfma_f32_16x16x32_bf16 v[60:63], v[140:143], v[92:95], v[60:63]\n"
    "ds_read_b128 v[100:103], v230 offset:40960\n"
    "v_mfma_f32_16x16x32_bf16 v[56:59], v[140:143], v[84:87], v[56:59]\n"
    "ds_read_b128 v[104:107], v230 offset:33792\n"
    "v_mfma_f32_16x16x32_bf16 v[56:59], v[136:139], v[80:83], v[56:59]\n"
    "ds_read_b128 v[108:111], v230 offset:41984\n"
    "v_mfma_f32_16x16x32_bf16 v[52:55], v[136:139], v[72:75], v[52:55]\n"
    "ds_read_b128 v[112:115], v230 offset:34816\n"
    "v_mfma_f32_16x16x32_bf16 v[52:55], v[140:143], v[76:79], v[52:55]\n"
    "ds_read_b128 v[116:119], v230 offset:43008\n"
    "v_mfma_f32_16x16x32_bf16 v[48:51], v[140:143], v[68:71], v[48:51]\n"
    "ds_read_b128 v[120:123], v230 offset:35840\n"
    "v_mfma_f32_16x16x32_bf16 v[48:51], v[136:139], v[64:67], v[48:51]\n"
    "ds_read_b128 v[124:127], v230 offset:44032\n"
    "v_mfma_f32_16x16x32_bf16 v[0:3], v[144:147], v[64:67], v[0:3]\n"
    "ds_read_b64_tr_b16 v[128:129], v231 offset:36864\n"
    "v_mfma_f32_16x16x32_bf16 v[0:3], v[148:151], v[68:71], v[0:3]\n"
    "ds_read_b64_tr_b16 v[130:131], v231 offset:38912\n"
    "v_mfma_f32_16x16x32_bf16 v[4:7], v[148:151], v[76:79], v[4:7]\n"
    "ds_read_b64_tr_b16 v[132:133], v231 offset:45056\n"
    "v_mfma_f32_16x16x32_bf16 v[4:7], v[144:147], v[72:75], v[4:7]\n"
    "ds_read_b64_tr_b16 v[134:135], v231 offset:47104\n"
    "v_mfma_f32_16x16x32_bf16 v[8:11], v[144:147], v[80:83], v[8:11]\n"
    "ds_read_b64_tr_b16 v[136:137], v232 offset:36864\n"
    "v_mfma_f32_16x16x32_bf16 v[8:11], v[148:151], v[84:87], v[8:11]\n"
    "ds_read_b64_tr_b16 v[138:139], v232 offset:38912\n"
    "v_mfma_f32_16x16x32_bf16 v[12:15], v[148:151], v[92:95], v[12:15]\n"
    "ds_read_b64_tr_b16 v[140:141], v232 offset:45056\n"
    "v_mfma_f32_16x16x32_bf16 v[12:15], v[144:147], v[88:91], v[12:15]\n"
    "ds_read_b64_tr_b16 v[142:143], v232 offset:47104\n"
    "s_barrier\n"
    "s_waitcnt lgkmcnt(4)\n"
    "v_mfma_f32_16x16x32_bf16 v[28:31], v[152:155], v[88:91], v[28:31]\n"
    "ds_read_b128 v[160:163], v230 offset:49152\n"
    "ds_read_b128 v[164:167], v230 offset:57344\n"
    "v_mfma_f32_16x16x32_bf16 v[28:31], v[156:159], v[92:95], v[28:31]\n"
    "ds_read_b64_tr_b16 v[192:193], v231 offset:53248\n"
    "ds_read_b64_tr_b16 v[194:195], v231 offset:55296\n"
    "v_mfma_f32_16x16x32_bf16 v[24:27], v[156:159], v[84:87], v[24:27]\n"
    "ds_read_b64_tr_b16 v[196:197], v231 offset:61440\n"
    "ds_read_b64_tr_b16 v[198:199], v231 offset:63488\n"
    "v_mfma_f32_16x16x32_bf16 v[24:27], v[152:155], v[80:83], v[24:27]\n"
    "ds_read_b64_tr_b16 v[200:201], v232 offset:53248\n"
    "ds_read_b64_tr_b16 v[202:203], v232 offset:55296\n"
    "v_mfma_f32_16x16x32_bf16 v[20:23], v[152:155], v[72:75], v[20:23]\n"
    "ds_read_b64_tr_b16 v[204:205], v232 offset:61440\n"
    "v_mfma_f32_16x16x32_bf16 v[20:23], v[156:159], v[76:79], v[20:23]\n"
    "ds_read_b64_tr_b16 v[206:207], v232 offset:63488\n"
    "v_mfma_f32_16x16x32_bf16 v[16:19], v[156:159], v[68:71], v[16:19]\n"
    "ds_read_b128 v[168:171], v230 offset:50176\n"
    "v_mfma_f32_16x16x32_bf16 v[16:19], v[152:155], v[64:67], v[16:19]\n"
    "ds_read_b128 v[172:175], v230 offset:58368\n"
    "v_mfma_f32_16x16x32_bf16 v[32:35], v[128:131], v[96:99], v[32:35]\n"
    "ds_read_b128 v[176:179], v230 offset:51200\n"
    "v_mfma_f32_16x16x32_bf16 v[32:35], v[132:135], v[100:103], v[32:35]\n"
    "ds_read_b128 v[180:183], v230 offset:59392\n"
    "s_waitcnt lgkmcnt(4)\n"
    "v_mfma_f32_16x16x32_bf16 v[32:35], v[192:195], v[160:163], v[32:35]\n"
    "ds_read_b128 v[184:187], v230 offset:52224\n"
    "v_mfma_f32_16x16x32_bf16 v[32:35], v[196:199], v[164:167], v[32:35]\n"
    "ds_read_b128 v[188:191], v230 offset:60416\n"
    "v_mfma_f32_16x16x32_bf16 v[48:51], v[136:139], v[96:99], v[48:51]\n"
    "ds_read_b64_tr_b16 v[144:145], v231 offset:32768\n"
    "v_mfma_f32_16x16x32_bf16 v[48:51], v[140:143], v[100:103], v[48:51]\n"
    "ds_read_b64_tr_b16 v[146:147], v231 offset:34816\n"
    "v_mfma_f32_16x16x32_bf16 v[48:51], v[200:203], v[160:163], v[48:51]\n"
    "ds_read_b64_tr_b16 v[148:149], v231 offset:40960\n"
    "v_mfma_f32_16x16x32_bf16 v[48:51], v[204:207], v[164:167], v[48:51]\n"
    "ds_read_b64_tr_b16 v[150:151], v231 offset:43008\n"
    "v_mfma_f32_16x16x32_bf16 v[52:55], v[136:139], v[104:107], v[52:55]\n"
    "ds_read_b64_tr_b16 v[152:153], v232 offset:32768\n"
    "v_mfma_f32_16x16x32_bf16 v[52:55], v[140:143], v[108:111], v[52:55]\n"
    "ds_read_b64_tr_b16 v[154:155], v232 offset:34816\n"
    "v_cvt_pk_bf16_f32 v208, v32, v33\n"
    "s_waitcnt lgkmcnt(4)\n"
    "v_mfma_f32_16x16x32_bf16 v[52:55], v[200:203], v[168:171], v[52:55]\n"
    "v_cvt_pk_bf16_f32 v209, v34, v35\n"
    "v_cvt_pk_bf16_f32 v210, v48, v49\n"
    "v_mfma_f32_16x16x32_bf16 v[52:55], v[204:207], v[172:175], v[52:55]\n"
    "ds_read_b64_tr_b16 v[156:157], v232 offset:40960\n"
    "v_cvt_pk_bf16_f32 v211, v50, v51\n"
    "v_mfma_f32_16x16x32_bf16 v[36:39], v[128:131], v[104:107], v[36:39]\n"
    "v_permlane16_swap_b32 v208, v210\n"
    "v_permlane16_swap_b32 v209, v211\n"
    "v_mfma_f32_16x16x32_bf16 v[36:39], v[132:135], v[108:111], v[36:39]\n"
    "v_min_u32 v208, -2, v208\n"
    "v_min_u32 v209, -2, v209\n"
    "v_mfma_f32_16x16x32_bf16 v[36:39], v[192:195], v[168:171], v[36:39]\n"
    "v_min_u32 v210, -2, v210\n"
    "v_min_u32 v211, -2, v211\n"
    "v_mfma_f32_16x16x32_bf16 v[36:39], v[196:199], v[172:175], v[36:39]\n"
    "ds_read_b64_tr_b16 v[158:159], v232 offset:43008\n"
    "buffer_store_dwordx4 v[208:211], v249, s[24:27], s55 offen offset:0 sc1\n"
    "v_mfma_f32_16x16x32_bf16 v[40:43], v[128:131], v[112:115], v[40:43]\n"
    "v_mfma_f32_16x16x32_bf16 v[40:43], v[132:135], v[116:119], v[40:43]\n"
    "s_nop 3\n"
    "v_cvt_pk_bf16_f32 v212, v36, v37\n"
    "v_cvt_pk_bf16_f32 v213, v38, v39\n"
    "v_mfma_f32_16x16x32_bf16 v[40:43], v[192:195], v[176:179], v[40:43]\n"
    "v_cvt_pk_bf16_f32 v214, v52, v53\n"
    "v_cvt_pk_bf16_f32 v215, v54, v55\n"
    "v_mfma_f32_16x16x32_bf16 v[40:43], v[196:199], v[180:183], v[40:43]\n"
    "v_permlane16_swap_b32 v212, v214\n"
    "v_permlane16_swap_b32 v213, v215\n"
    "v_mfma_f32_16x16x32_bf16 v[56:59], v[136:139], v[112:115], v[56:59]\n"
    "v_min_u32 v212, -2, v212\n"
    "v_min_u32 v213, -2, v213\n"
    "v_mfma_f32_16x16x32_bf16 v[56:59], v[140:143], v[116:119], v[56:59]\n"
    "v_min_u32 v214, -2, v214\n"
    "v_min_u32 v215, -2, v215\n"
    "v_mfma_f32_16x16x32_bf16 v[56:59], v[200:203], v[176:179], v[56:59]\n"
    "buffer_store_dwordx4 v[212:215], v249, s[24:27], s55 offen offset:1024 sc1\n"
    "v_mfma_f32_16x16x32_bf16 v[56:59], v[204:207], v[180:183], v[56:59]\n"
    "v_mfma_f32_16x16x32_bf16 v[60:63], v[136:139], v[120:123], v[60:63]\n"
    "v_mfma_f32_16x16x32_bf16 v[60:63], v[140:143], v[124:127], v[60:63]\n"
    "v_cvt_pk_bf16_f32 v216, v40, v41\n"
    "v_cvt_pk_bf16_f32 v217, v42, v43\n"
    "v_mfma_f32_16x16x32_bf16 v[60:63], v[200:203], v[184:187], v[60:63]\n"
    "s_nop 2\n"
    "v_cvt_pk_bf16_f32 v218, v56, v57\n"
    "v_cvt_pk_bf16_f32 v219, v58, v59\n"
    "v_mfma_f32_16x16x32_bf16 v[60:63], v[204:207], v[188:191], v[60:63]\n"
    "v_permlane16_swap_b32 v216, v218\n"
    "v_permlane16_swap_b32 v217, v219\n"
    "v_mfma_f32_16x16x32_bf16 v[44:47], v[128:131], v[120:123], v[44:47]\n"
    "v_min_u32 v216, -2, v216\n"
    "v_min_u32 v217, -2, v217\n"
    "v_mfma_f32_16x16x32_bf16 v[44:47], v[132:135], v[124:127], v[44:47]\n"
    "v_min_u32 v218, -2, v218\n"
    "v_min_u32 v219, -2, v219\n"
    "v_mfma_f32_16x16x32_bf16 v[44:47], v[192:195], v[184:187], v[44:47]\n"
    "buffer_store_dwordx4 v[216:219], v249, s[24:27], s55 offen offset:2048 sc1\n"
    "v_mfma_f32_16x16x32_bf16 v[44:47], v[196:199], v[188:191], v[44:47]\n"
    "v_mfma_f32_16x16x32_bf16 v[0:3], v[144:147], v[96:99], v[0:3]\n"
    "ds_read_b64_tr_b16 v[128:129], v231 offset:49152\n"
    "s_waitcnt lgkmcnt(1)\n"
    "v_mfma_f32_16x16x32_bf16 v[0:3], v[148:151], v[100:103], v[0:3]\n"
    "ds_read_b64_tr_b16 v[130:131], v231 offset:51200\n"
    "s_nop 2\n"
    "v_cvt_pk_bf16_f32 v220, v44, v45\n"
    "v_mfma_f32_16x16x32_bf16 v[4:7], v[148:151], v[108:111], v[4:7]\n"
    "ds_read_b64_tr_b16 v[132:133], v231 offset:57344\n"
    "v_cvt_pk_bf16_f32 v221, v46, v47\n"
    "v_mfma_f32_16x16x32_bf16 v[4:7], v[144:147], v[104:107], v[4:7]\n"
    "ds_read_b64_tr_b16 v[134:135], v231 offset:59392\n"
    "v_cvt_pk_bf16_f32 v222, v60, v61\n"
    "v_mfma_f32_16x16x32_bf16 v[8:11], v[144:147], v[112:115], v[8:11]\n"
    "ds_read_b64_tr_b16 v[136:137], v232 offset:49152\n"
    "v_cvt_pk_bf16_f32 v223, v62, v63\n"
    "v_mfma_f32_16x16x32_bf16 v[8:11], v[148:151], v[116:119], v[8:11]\n"
    "ds_read_b64_tr_b16 v[138:139], v232 offset:51200\n"
    "v_permlane16_swap_b32 v220, v222\n"
    "v_mfma_f32_16x16x32_bf16 v[12:15], v[148:151], v[124:127], v[12:15]\n"
    "ds_read_b64_tr_b16 v[140:141], v232 offset:57344\n"
    "v_permlane16_swap_b32 v221, v223\n"
    "v_mfma_f32_16x16x32_bf16 v[12:15], v[144:147], v[120:123], v[12:15]\n"
    "ds_read_b64_tr_b16 v[142:143], v232 offset:59392\n"
    "v_min_u32 v220, -2, v220\n"
    "v_mfma_f32_16x16x32_bf16 v[28:31], v[152:155], v[120:123], v[28:31]\n"
    "v_min_u32 v221, -2, v221\n"
    "v_min_u32 v222, -2, v222\n"
    "v_mfma_f32_16x16x32_bf16 v[28:31], v[156:159], v[124:127], v[28:31]\n"
    "v_min_u32 v223, -2, v223\n"
    "buffer_store_dwordx4 v[220:223], v249, s[24:27], s55 offen offset:3072 sc1\n"
    "s_barrier\n"
    "v_xor_b32 v226, 0, v238\n"
    "v_mfma_f32_16x16x32_bf16 v[24:27], v[156:159], v[116:119], v[24:27]\n"
    "ds_read_b128 v[192:195], v239 offset:0\n"
    "ds_read_b128 v[208:211], v240 offset:0\n"
    "v_mfma_f32_16x16x32_bf16 v[24:27], v[152:155], v[112:115], v[24:27]\n"
    "ds_read_b64_tr_b16 v[64:65], v226 offset:0\n"
    "ds_read_b64_tr_b16 v[66:67], v226 offset:1024\n"
    "v_mfma_f32_16x16x32_bf16 v[20:23], v[152:155], v[104:107], v[20:23]\n"
    "v_xor_b32 v226, 32, v238\n"
    "ds_read_b128 v[196:199], v239 offset:2048\n"
    "v_mfma_f32_16x16x32_bf16 v[20:23], v[156:159], v[108:111], v[20:23]\n"
    "ds_read_b128 v[212:215], v240 offset:256\n"
    "ds_read_b64_tr_b16 v[68:69], v226 offset:0\n"
    "v_mfma_f32_16x16x32_bf16 v[16:19], v[156:159], v[100:103], v[16:19]\n"
    "ds_read_b64_tr_b16 v[70:71], v226 offset:1024\n"
    "v_xor_b32 v226, 64, v238\n"
    "v_mfma_f32_16x16x32_bf16 v[16:19], v[152:155], v[96:99], v[16:19]\n"
    "ds_read_b128 v[200:203], v239 offset:4096\n"
    "ds_read_b128 v[216:219], v240 offset:512\n"
    "s_waitcnt lgkmcnt(6)\n"
    "v_mfma_f32_16x16x32_bf16 v[0:3], v[128:131], v[160:163], v[0:3]\n"
    "ds_read_b64_tr_b16 v[72:73], v226 offset:0\n"
    "ds_read_b64_tr_b16 v[74:75], v226 offset:1024\n"
    "v_mfma_f32_16x16x32_bf16 v[0:3], v[132:135], v[164:167], v[0:3]\n"
    "v_xor_b32 v226, 96, v238\n"
    "ds_read_b128 v[204:207], v239 offset:6144\n"
    "v_mfma_f32_16x16x32_bf16 v[4:7], v[132:135], v[172:175], v[4:7]\n"
    "ds_read_b128 v[220:223], v240 offset:768\n"
    "ds_read_b64_tr_b16 v[76:77], v226 offset:0\n"
    "v_mfma_f32_16x16x32_bf16 v[4:7], v[128:131], v[168:171], v[4:7]\n"
    "ds_read_b64_tr_b16 v[78:79], v226 offset:1024\n"
    "v_mov_b32 v224, 0xffff0000\n"
    "v_mfma_f32_16x16x32_bf16 v[8:11], v[128:131], v[176:179], v[8:11]\n"
    "v_mov_b32 v225, 0xbf317218\n"
    "v_mul_f32 v208, v208, v210\n"
    "v_mfma_f32_16x16x32_bf16 v[8:11], v[132:135], v[180:183], v[8:11]\n"
    "v_lshlrev_b32 v32, 16, v192\n"
    "v_and_b32 v33, v224, v192\n"
    "v_mfma_f32_16x16x32_bf16 v[12:15], v[132:135], v[188:191], v[12:15]\n"
    "v_lshlrev_b32 v34, 16, v193\n"
    "v_and_b32 v35, v224, v193\n"
    "v_mfma_f32_16x16x32_bf16 v[12:15], v[128:131], v[184:187], v[12:15]\n"
    "v_lshlrev_b32 v36, 16, v194\n"
    "v_and_b32 v37, v224, v194\n"
    "v_mfma_f32_16x16x32_bf16 v[28:31], v[136:139], v[184:187], v[28:31]\n"
    "v_lshlrev_b32 v38, 16, v195\n"
    "v_and_b32 v39, v224, v195\n"
    "v_mfma_f32_16x16x32_bf16 v[28:31], v[140:143], v[188:191], v[28:31]\n"
    "v_lshlrev_b32 v80, 16, v64\n"
    "v_and_b32 v81, v224, v64\n"
    "v_mfma_f32_16x16x32_bf16 v[24:27], v[140:143], v[180:183], v[24:27]\n"
    "v_lshlrev_b32 v82, 16, v65\n"
    "v_and_b32 v83, v224, v65\n"
    "v_mfma_f32_16x16x32_bf16 v[24:27], v[136:139], v[176:179], v[24:27]\n"
    "v_lshlrev_b32 v84, 16, v66\n"
    "v_and_b32 v85, v224, v66\n"
    "v_mfma_f32_16x16x32_bf16 v[20:23], v[136:139], v[168:171], v[20:23]\n"
    "v_lshlrev_b32 v86, 16, v67\n"
    "v_and_b32 v87, v224, v67\n"
    "v_mfma_f32_16x16x32_bf16 v[20:23], v[140:143], v[172:175], v[20:23]\n"
    "v_fma_f32 v88, v32, v210, v208\n"
    "v_fma_f32 v89, v33, v210, v208\n"
    "v_mfma_f32_16x16x32_bf16 v[16:19], v[140:143], v[164:167], v[16:19]\n"
    "v_fma_f32 v90, v34, v210, v208\n"
    "v_fma_f32 v91, v35, v210, v208\n"
    "v_mfma_f32_16x16x32_bf16 v[16:19], v[136:139], v[160:163], v[16:19]\n"
    "v_fma_f32 v92, v36, v210, v208\n"
    "v_fma_f32 v93, v37, v210, v208\n"
    "buffer_load_dwordx4 v[144:147], v249, s[24:27], s56 offen offset:0 sc1\n"
    "buffer_load_dwordx4 v[148:151], v249, s[24:27], s56 offen offset:1024 sc1\n"
    "buffer_load_dwordx4 v[152:155], v249, s[24:27], s56 offen offset:2048 sc1\n"
    "buffer_load_dwordx4 v[156:159], v249, s[24:27], s56 offen offset:3072 sc1\n"
    "v_fma_f32 v94, v38, v210, v208\n"
    "v_fma_f32 v95, v39, v210, v208\n"
    "v_fmac_f32 v32, v88, v80\n"
    "v_fmac_f32 v33, v89, v81\n"
    "v_fmac_f32 v34, v90, v82\n"
    "v_fmac_f32 v35, v91, v83\n"
    "v_fmac_f32 v36, v92, v84\n"
    "v_fmac_f32 v37, v93, v85\n"
    "v_fmac_f32 v38, v94, v86\n"
    "v_fmac_f32 v39, v95, v87\n"
    "v_permlane16_swap_b32 v0, v16\n"
    "v_permlane16_swap_b32 v1, v17\n"
    "v_permlane16_swap_b32 v2, v18\n"
    "v_permlane16_swap_b32 v3, v19\n"
    "v_pk_fma_f32 v[32:33], v[224:225], v[0:1], v[32:33] op_sel:[1,0,0]\n"
    "v_pk_fma_f32 v[34:35], v[224:225], v[2:3], v[34:35] op_sel:[1,0,0]\n"
    "v_pk_fma_f32 v[36:37], v[224:225], v[16:17], v[36:37] op_sel:[1,0,0]\n"
    "v_pk_fma_f32 v[38:39], v[224:225], v[18:19], v[38:39] op_sel:[1,0,0]\n"
    "s_waitcnt lgkmcnt(10)\n"
    "v_mul_f32 v212, v212, v214\n"
    "v_lshlrev_b32 v40, 16, v196\n"
    "v_and_b32 v41, v224, v196\n"
    "v_lshlrev_b32 v42, 16, v197\n"
    "v_and_b32 v43, v224, v197\n"
    "v_lshlrev_b32 v44, 16, v198\n"
    "v_and_b32 v45, v224, v198\n"
    "v_lshlrev_b32 v46, 16, v199\n"
    "v_and_b32 v47, v224, v199\n"
    "s_waitcnt lgkmcnt(9)\n"
    "v_lshlrev_b32 v80, 16, v68\n"
    "v_and_b32 v81, v224, v68\n"
    "v_lshlrev_b32 v82, 16, v69\n"
    "v_and_b32 v83, v224, v69\n"
    "s_waitcnt lgkmcnt(8)\n"
    "v_lshlrev_b32 v84, 16, v70\n"
    "v_and_b32 v85, v224, v70\n"
    "v_lshlrev_b32 v86, 16, v71\n"
    "v_and_b32 v87, v224, v71\n"
    "v_pk_fma_f32 v[88:89], v[40:41], v[214:215], v[212:213] op_sel_hi:[1,0,0]\n"
    "v_pk_fma_f32 v[90:91], v[42:43], v[214:215], v[212:213] op_sel_hi:[1,0,0]\n"
    "v_pk_fma_f32 v[92:93], v[44:45], v[214:215], v[212:213] op_sel_hi:[1,0,0]\n"
    "v_pk_fma_f32 v[94:95], v[46:47], v[214:215], v[212:213] op_sel_hi:[1,0,0]\n"
    "v_pk_fma_f32 v[40:41], v[88:89], v[80:81], v[40:41]\n"
    "v_pk_fma_f32 v[42:43], v[90:91], v[82:83], v[42:43]\n"
    "v_pk_fma_f32 v[44:45], v[92:93], v[84:85], v[44:45]\n"
    "v_pk_fma_f32 v[46:47], v[94:95], v[86:87], v[46:47]\n"
    "v_permlane16_swap_b32 v4, v20\n"
    "v_permlane16_swap_b32 v5, v21\n"
    "v_permlane16_swap_b32 v6, v22\n"
    "v_permlane16_swap_b32 v7, v23\n"
    "v_pk_fma_f32 v[40:41], v[224:225], v[4:5], v[40:41] op_sel:[1,0,0]\n"
    "v_pk_fma_f32 v[42:43], v[224:225], v[6:7], v[42:43] op_sel:[1,0,0]\n"
    "v_pk_fma_f32 v[44:45], v[224:225], v[20:21], v[44:45] op_sel:[1,0,0]\n"
    "v_pk_fma_f32 v[46:47], v[224:225], v[22:23], v[46:47] op_sel:[1,0,0]\n"
    "s_waitcnt lgkmcnt(6)\n"
    "v_mul_f32 v216, v216, v218\n"
    "v_lshlrev_b32 v48, 16, v200\n"
    "v_and_b32 v49, v224, v200\n"
    "v_lshlrev_b32 v50, 16, v201\n"
    "v_and_b32 v51, v224, v201\n"
    "v_lshlrev_b32 v52, 16, v202\n"
    "v_and_b32 v53, v224, v202\n"
    "v_lshlrev_b32 v54, 16, v203\n"
    "v_and_b32 v55, v224, v203\n"
    "s_waitcnt lgkmcnt(5)\n"
    "v_lshlrev_b32 v80, 16, v72\n"
    "v_and_b32 v81, v224, v72\n"
    "v_lshlrev_b32 v82, 16, v73\n"
    "v_and_b32 v83, v224, v73\n"
    "s_waitcnt lgkmcnt(4)\n"
    "v_lshlrev_b32 v84, 16, v74\n"
    "v_and_b32 v85, v224, v74\n"
    "v_lshlrev_b32 v86, 16, v75\n"
    "v_and_b32 v87, v224, v75\n"
    "v_pk_fma_f32 v[88:89], v[48:49], v[218:219], v[216:217] op_sel_hi:[1,0,0]\n"
    "v_pk_fma_f32 v[90:91], v[50:51], v[218:219], v[216:217] op_sel_hi:[1,0,0]\n"
    "v_pk_fma_f32 v[92:93], v[52:53], v[218:219], v[216:217] op_sel_hi:[1,0,0]\n"
    "v_pk_fma_f32 v[94:95], v[54:55], v[218:219], v[216:217] op_sel_hi:[1,0,0]\n"
    "v_pk_fma_f32 v[48:49], v[88:89], v[80:81], v[48:49]\n"
    "v_pk_fma_f32 v[50:51], v[90:91], v[82:83], v[50:51]\n"
    "v_pk_fma_f32 v[52:53], v[92:93], v[84:85], v[52:53]\n"
    "v_pk_fma_f32 v[54:55], v[94:95], v[86:87], v[54:55]\n"
    "v_permlane16_swap_b32 v8, v24\n"
    "v_permlane16_swap_b32 v9, v25\n"
    "v_permlane16_swap_b32 v10, v26\n"
    "v_permlane16_swap_b32 v11, v27\n"
    "v_pk_fma_f32 v[48:49], v[224:225], v[8:9], v[48:49] op_sel:[1,0,0]\n"
    "v_pk_fma_f32 v[50:51], v[224:225], v[10:11], v[50:51] op_sel:[1,0,0]\n"
    "v_pk_fma_f32 v[52:53], v[224:225], v[24:25], v[52:53] op_sel:[1,0,0]\n"
    "v_pk_fma_f32 v[54:55], v[224:225], v[26:27], v[54:55] op_sel:[1,0,0]\n"
    "s_waitcnt lgkmcnt(2)\n"
    "v_mul_f32 v220, v220, v222\n"
    "v_lshlrev_b32 v56, 16, v204\n"
    "v_and_b32 v57, v224, v204\n"
    "v_lshlrev_b32 v58, 16, v205\n"
    "v_and_b32 v59, v224, v205\n"
    "v_lshlrev_b32 v60, 16, v206\n"
    "v_and_b32 v61, v224, v206\n"
    "v_lshlrev_b32 v62, 16, v207\n"
    "v_and_b32 v63, v224, v207\n"
    "s_waitcnt lgkmcnt(1)\n"
    "v_lshlrev_b32 v80, 16, v76\n"
    "v_and_b32 v81, v224, v76\n"
    "v_lshlrev_b32 v82, 16, v77\n"
    "v_and_b32 v83, v224, v77\n"
    "s_waitcnt lgkmcnt(0)\n"
    "v_lshlrev_b32 v84, 16, v78\n"
    "v_and_b32 v85, v224, v78\n"
    "v_lshlrev_b32 v86, 16, v79\n"
    "v_and_b32 v87, v224, v79\n"
    "v_pk_fma_f32 v[88:89], v[56:57], v[222:223], v[220:221] op_sel_hi:[1,0,0]\n"
    "v_pk_fma_f32 v[90:91], v[58:59], v[222:223], v[220:221] op_sel_hi:[1,0,0]\n"
    "v_pk_fma_f32 v[92:93], v[60:61], v[222:223], v[220:221] op_sel_hi:[1,0,0]\n"
    "v_pk_fma_f32 v[94:95], v[62:63], v[222:223], v[220:221] op_sel_hi:[1,0,0]\n"
    "v_pk_fma_f32 v[56:57], v[88:89], v[80:81], v[56:57]\n"
    "v_pk_fma_f32 v[58:59], v[90:91], v[82:83], v[58:59]\n"
    "v_pk_fma_f32 v[60:61], v[92:93], v[84:85], v[60:61]\n"
    "v_pk_fma_f32 v[62:63], v[94:95], v[86:87], v[62:63]\n"
    "v_permlane16_swap_b32 v12, v28\n"
    "v_permlane16_swap_b32 v13, v29\n"
    "v_permlane16_swap_b32 v14, v30\n"
    "v_permlane16_swap_b32 v15, v31\n"
    "v_pk_fma_f32 v[56:57], v[224:225], v[12:13], v[56:57] op_sel:[1,0,0]\n"
    "v_pk_fma_f32 v[58:59], v[224:225], v[14:15], v[58:59] op_sel:[1,0,0]\n"
    "v_pk_fma_f32 v[60:61], v[224:225], v[28:29], v[60:61] op_sel:[1,0,0]\n"
    "v_pk_fma_f32 v[62:63], v[224:225], v[30:31], v[62:63] op_sel:[1,0,0]\n"
    "s_waitcnt vmcnt(3)\n"
    "v_max3_u32 v120, v144, v145, v146\n"
    "v_max_u32 v120, v120, v147\n"
    "v_cmp_eq_u32 vcc, -1, v120\n"
    "s_cbranch_vccnz Ls18_%=\n"
    "Lx19_%=:\n"
    "v_lshlrev_b32 v120, 16, v144\n"
    "v_and_b32 v121, v224, v144\n"
    "v_lshlrev_b32 v122, 16, v145\n"
    "v_and_b32 v123, v224, v145\n"
    "v_lshlrev_b32 v124, 16, v146\n"
    "v_and_b32 v125, v224, v146\n"
    "v_lshlrev_b32 v126, 16, v147\n"
    "v_and_b32 v127, v224, v147\n"
    "v_pk_fma_f32 v[32:33], v[224:225], v[120:121], v[32:33] op_sel:[1,0,0]\n"
    "v_pk_fma_f32 v[34:35], v[224:225], v[122:123], v[34:35] op_sel:[1,0,0]\n"
    "v_pk_fma_f32 v[36:37], v[224:225], v[124:125], v[36:37] op_sel:[1,0,0]\n"
    "v_pk_fma_f32 v[38:39], v[224:225], v[126:127], v[38:39] op_sel:[1,0,0]\n"
    "v_cvt_pk_bf16_f32 v96, v32, v33\n"
    "v_cvt_pk_bf16_f32 v97, v34, v35\n"
    "v_cvt_pk_bf16_f32 v98, v36, v37\n"
    "v_cvt_pk_bf16_f32 v99, v38, v39\n"
    "s_mov_b32 s50, 0\n"
    "buffer_store_dwordx4 v[96:99], v241, s[36:39], s50 offen\n"
    "s_waitcnt vmcnt(3)\n"
    "v_max3_u32 v120, v148, v149, v150\n"
    "v_max_u32 v120, v120, v151\n"
    "v_cmp_eq_u32 vcc, -1, v120\n"
    "s_cbranch_vccnz Ls20_%=\n"
    "Lx21_%=:\n"
    "v_lshlrev_b32 v120, 16, v148\n"
    "v_and_b32 v121, v224, v148\n"
    "v_lshlrev_b32 v122, 16, v149\n"
    "v_and_b32 v123, v224, v149\n"
    "v_lshlrev_b32 v124, 16, v150\n"
    "v_and_b32 v125, v224, v150\n"
    "v_lshlrev_b32 v126, 16, v151\n"
    "v_and_b32 v127, v224, v151\n"
    "v_pk_fma_f32 v[40:41], v[224:225], v[120:121], v[40:41] op_sel:[1,0,0]\n"
    "v_pk_fma_f32 v[42:43], v[224:225], v[122:123], v[42:43] op_sel:[1,0,0]\n"
    "v_pk_fma_f32 v[44:45], v[224:225], v[124:125], v[44:45] op_sel:[1,0,0]\n"
    "v_pk_fma_f32 v[46:47], v[224:225], v[126:127], v[46:47] op_sel:[1,0,0]\n"
    "v_cvt_pk_bf16_f32 v100, v40, v41\n"
    "v_cvt_pk_bf16_f32 v101, v42, v43\n"
    "v_cvt_pk_bf16_f32 v102, v44, v45\n"
    "v_cvt_pk_bf16_f32 v103, v46, v47\n"
    "s_mov_b32 s50, 32768\n"
    "buffer_store_dwordx4 v[100:103], v241, s[36:39], s50 offen\n"
    "s_waitcnt vmcnt(3)\n"
    "v_max3_u32 v120, v152, v153, v154\n"
    "v_max_u32 v120, v120, v155\n"
    "v_cmp_eq_u32 vcc, -1, v120\n"
    "s_cbranch_vccnz Ls22_%=\n"
    "Lx23_%=:\n"
    "v_lshlrev_b32 v120, 16, v152\n"
    "v_and_b32 v121, v224, v152\n"
    "v_lshlrev_b32 v122, 16, v153\n"
    "v_and_b32 v123, v224, v153\n"
    "v_lshlrev_b32 v124, 16, v154\n"
    "v_and_b32 v125, v224, v154\n"
    "v_lshlrev_b32 v126, 16, v155\n"
    "v_and_b32 v127, v224, v155\n"
    "v_pk_fma_f32 v[48:49], v[224:225], v[120:121], v[48:49] op_sel:[1,0,0]\n"
    "v_pk_fma_f32 v[50:51], v[224:225], v[122:123], v[50:51] op_sel:[1,0,0]\n"
    "v_pk_fma_f32 v[52:53], v[224:225], v[124:125], v[52:53] op_sel:[1,0,0]\n"
    "v_pk_fma_f32 v[54:55], v[224:225], v[126:127], v[54:55] op_sel:[1,0,0]\n"
    "v_cvt_pk_bf16_f32 v104, v48, v49\n"
    "v_cvt_pk_bf16_f32 v105, v50, v51\n"
    "v_cvt_pk_bf16_f32 v106, v52, v53\n"
    "v_cvt_pk_bf16_f32 v107, v54, v55\n"
    "s_mov_b32 s50, 65536\n"
    "buffer_store_dwordx4 v[104:107], v241, s[36:39], s50 offen\n"
    "s_waitcnt vmcnt(3)\n"
    "v_max3_u32 v120, v156, v157, v158\n"
    "v_max_u32 v120, v120, v159\n"
    "v_cmp_eq_u32 vcc, -1, v120\n"
    "s_cbranch_vccnz Ls24_%=\n"
    "Lx25_%=:\n"
    "v_lshlrev_b32 v120, 16, v156\n"
    "v_and_b32 v121, v224, v156\n"
    "v_lshlrev_b32 v122, 16, v157\n"
    "v_and_b32 v123, v224, v157\n"
    "v_lshlrev_b32 v124, 16, v158\n"
    "v_and_b32 v125, v224, v158\n"
    "v_lshlrev_b32 v126, 16, v159\n"
    "v_and_b32 v127, v224, v159\n"
    "v_pk_fma_f32 v[56:57], v[224:225], v[120:121], v[56:57] op_sel:[1,0,0]\n"
    "v_pk_fma_f32 v[58:59], v[224:225], v[122:123], v[58:59] op_sel:[1,0,0]\n"
    "v_pk_fma_f32 v[60:61], v[224:225], v[124:125], v[60:61] op_sel:[1,0,0]\n"
    "v_pk_fma_f32 v[62:63], v[224:225], v[126:127], v[62:63] op_sel:[1,0,0]\n"
    "v_cvt_pk_bf16_f32 v108, v56, v57\n"
    "v_cvt_pk_bf16_f32 v109, v58, v59\n"
    "v_cvt_pk_bf16_f32 v110, v60, v61\n"
    "v_cvt_pk_bf16_f32 v111, v62, v63\n"
    "s_mov_b32 s50, 98304\n"
    "buffer_store_dwordx4 v[108:111], v241, s[36:39], s50 offen\n"
    "s_endpgm\n"
    "Ls6_%=:\n"
    "s_cmp_lg_u32 s70, 0\n"
    "s_cbranch_scc1 Lnb4_%=\n"
    "s_mov_b64 exec, 1\n"
    "v_mov_b32 v247, 1\n"
    "buffer_atomic_add v247, v250, s[24:27], 0 offen sc1\n"
    "s_mov_b64 exec, -1\n"
    "Lnb4_%=:\n"
    "s_sleep 2\n"
    "s_sub_u32 s47, s47, 1\n"
    "s_cbranch_scc1 Lexit_%=\n"
    "s_branch Lg5_%=\n"
    "Lh2_%=:\n"
    "Lg5_%=:\n"
    "buffer_load_dword v247, v250, s[24:27], 0 offen sc1\n"
    "s_waitcnt vmcnt(0)\n"
    "v_cmp_eq_u32 vcc, v251, v247\n"
    "s_cbranch_vccnz Ls6_%=\n"
    "s_branch Lb3_%=\n"
    "Ls12_%=:\n"
    "s_cmp_lg_u32 s70, 0\n"
    "s_cbranch_scc1 Lnb10_%=\n"
    "s_mov_b64 exec, 1\n"
    "v_mov_b32 v247, 1\n"
    "buffer_atomic_add v247, v250, s[24:27], 0 offen sc1\n"
    "s_mov_b64 exec, -1\n"
    "Lnb10_%=:\n"
    "s_sleep 2\n"
    "s_sub_u32 s47, s47, 1\n"
    "s_cbranch_scc1 Lexit_%=\n"
    "s_branch Lg11_%=\n"
    "Lh8_%=:\n"
    "Lg11_%=:\n"
    "buffer_load_dword v247, v253, s[24:27], 0 offen sc1\n"
    "s_waitcnt vmcnt(0)\n"
    "v_cmp_gt_u32 vcc, 4, v247\n"
    "s_cbranch_vccnz Ls12_%=\n"
    "s_branch Lb9_%=\n"
    "Lc14_%=:\n"
    "Lt16_%=:\n"
    "ds_read_b32 v247, v245 offset:0\n"
    "s_waitcnt lgkmcnt(0)\n"
    "v_cmp_gt_u32 vcc, s59, v247\n"
    "s_cbranch_vccz Lb15_%=\n"
    "s_sub_u32 s47, s47, 1\n"
    "s_cbranch_scc0 Lt16_%=\n"
    "s_endpgm\n"
    "Ls18_%=:\n"
    "buffer_load_dwordx4 v[144:147], v249, s[24:27], s56 offen offset:0 sc1\n"
    "buffer_load_dwordx4 v[148:151], v249, s[24:27], s56 offen offset:1024 sc1\n"
    "buffer_load_dwordx4 v[152:155], v249, s[24:27], s56 offen offset:2048 sc1\n"
    "buffer_load_dwordx4 v[156:159], v249, s[24:27], s56 offen offset:3072 sc1\n"
    "s_sub_u32 s47, s47, 1\n"
    "s_cbranch_scc1 Lexit_%=\n"
    "s_waitcnt vmcnt(0)\n"
    "v_max3_u32 v120, v144, v145, v146\n"
    "v_max_u32 v120, v120, v147\n"
    "v_cmp_eq_u32 vcc, -1, v120\n"
    "s_cbranch_vccnz Ls18_%=\n"
    "s_branch Lx19_%=\n"
    "Ls20_%=:\n"
    "buffer_load_dwordx4 v[148:151], v249, s[24:27], s56 offen offset:1024 sc1\n"
    "buffer_load_dwordx4 v[152:155], v249, s[24:27], s56 offen offset:2048 sc1\n"
    "buffer_load_dwordx4 v[156:159], v249, s[24:27], s56 offen offset:3072 sc1\n"
    "s_sub_u32 s47, s47, 1\n"
    "s_cbranch_scc1 Lexit_%=\n"
    "s_waitcnt vmcnt(0)\n"
    "v_max3_u32 v120, v148, v149, v150\n"
    "v_max_u32 v120, v120, v151\n"
    "v_cmp_eq_u32 vcc, -1, v120\n"
    "s_cbranch_vccnz Ls20_%=\n"
    "s_branch Lx21_%=\n"
    "Ls22_%=:\n"
    "buffer_load_dwordx4 v[152:155], v249, s[24:27], s56 offen offset:2048 sc1\n"
    "buffer_load_dwordx4 v[156:159], v249, s[24:27], s56 offen offset:3072 sc1\n"
    "s_sub_u32 s47, s47, 1\n"
    "s_cbranch_scc1 Lexit_%=\n"
    "s_waitcnt vmcnt(0)\n"
    "v_max3_u32 v120, v152, v153, v154\n"
    "v_max_u32 v120, v120, v155\n"
    "v_cmp_eq_u32 vcc, -1, v120\n"
    "s_cbranch_vccnz Ls22_%=\n"
    "s_branch Lx23_%=\n"
    "Ls24_%=:\n"
    "buffer_load_dwordx4 v[156:159], v249, s[24:27], s56 offen offset:3072 sc1\n"
    "s_sub_u32 s47, s47, 1\n"
    "s_cbranch_scc1 Lexit_%=\n"
    "s_waitcnt vmcnt(0)\n"
    "v_max3_u32 v120, v156, v157, v158\n"
    "v_max_u32 v120, v120, v159\n"
    "v_cmp_eq_u32 vcc, -1, v120\n"
    "s_cbranch_vccnz Ls24_%=\n"
    "s_branch Lx25_%=\n"
    "LG0_%=:\n"
    "v_mov_b32 v255, v0\n"
    "v_lshlrev_b32 v222, 4, v0\n"
    "v_lshlrev_b32 v248, 2, v0\n"
    "v_and_b32 v218, 12, v248\n"
    "v_add_u32 v218, 155264, v218\n"
    "s_lshl_b32 s53, s40, 8\n"
    "s_lshl2_add_u32 s50, s40, 155264\n"
    "v_mov_b32 v219, s50\n"
    "v_mov_b32 v223, 0\n"
    "v_mov_b32 v221, 0\n"
    "ds_write_b32 v219, v223 offset:0\n"
    "ds_write_b32 v219, v223 offset:16\n"
    "ds_write_b32 v219, v223 offset:32\n"
    "ds_write_b32 v219, v223 offset:48\n"
    "s_mul_i32 s50, s40, 160\n"
    "s_add_u32 s50, s50, 135168\n"
    "v_add_u32 v249, s50, v248\n"
    "s_mov_b32 exec_hi, 0xff\n"
    "ds_write_b32 v249, v223\n"
    "s_mov_b32 exec_hi, -1\n"
    "s_lshr_b32 s54, s2, 3\n"
    "s_lshl2_add_u32 s54, s54, s40\n"
    "s_and_b32 s55, s54, 7\n"
    "s_lshl_b32 s55, s55, 2\n"
    "s_lshr_b32 s55, 0x18764210, s55\n"
    "s_and_b32 s55, s55, 15\n"
    "s_lshr_b32 s54, s54, 3\n"
    "s_lshl_b32 s54, s54, 21\n"
    "s_lshl_b32 s56, s55, 3\n"
    "s_load_dwordx2 s[68:69], s[0:1], s56\n"
    "s_and_b32 s56, s55, 14\n"
    "s_cmp_eq_u32 s56, 6\n"
    "s_cselect_b32 s56, 3145728, 0\n"
    "s_cmp_eq_u32 s55, 8\n"
    "s_cselect_b32 s56, 23592960, s56\n"
    "s_add_u32 s56, s56, 4194300\n"
    "s_min_u32 s59, s54, s56\n"
    "s_waitcnt lgkmcnt(0)\n"
    "s_lshl2_add_u32 s50, s2, s40\n"
    "s_lshl_b32 s50, s50, 12\n"
    "v_add_u32 v249, s50, v222\n"
    "global_load_dwordx4 v[160:163], v249, s[4:5] offset:0\n"
    "global_load_dwordx4 v[164:167], v249, s[4:5] offset:1024\n"
    "global_load_dwordx4 v[168:171], v249, s[4:5] offset:2048\n"
    "global_load_dwordx4 v[172:175], v249, s[4:5] offset:3072\n"
    "s_load_dword s59, s[68:69], s59\n"
    "s_load_dword s68, s[10:11], 0\n"
    "s_mov_b32 s24, s20\n"
    "s_and_b32 s25, s21, 0xffff\n"
    "s_mov_b32 s26, 27787264\n"
    "s_mov_b32 s27, 0x20000\n"
    "s_lshl4_add_u32 s3, s41, s42\n"
    "s_lshl_b32 s3, s3, 7\n"
    "s_add_u32 s3, s3, 32768\n"
    "s_lshl_b32 s43, s2, 7\n"
    "s_add_u32 s43, s43, 65536\n"
    "v_mov_b32 v244, 0\n"
    "v_mov_b32 v245, 0\n"
    "v_mov_b32 v246, 0\n"
    "v_mov_b32 v247, 0\n"
    "s_cmp_eq_u32 s49, 0\n"
    "s_cselect_b32 s50, s3, s43\n"
    "s_lshl_b32 s51, s48, 28\n"
    "v_add_u32 v250, s51, v222\n"
    "s_mov_b64 exec, 15\n"
    "buffer_store_dwordx4 v[244:247], v250, s[24:27], s50 offen sc1\n"
    "s_mov_b64 exec, -1\n"
    "v_and_b32 v248, 15, v255\n"
    "v_lshrrev_b32 v249, 4, v255\n"
    "v_bfe_u32 v250, v255, 2, 2\n"
    "v_and_b32 v251, 3, v255\n"
    "v_lshrrev_b32 v252, 1, v248\n"
    "v_xor_b32 v252, v252, v249\n"
    "v_lshlrev_b32 v252, 4, v252\n"
    "v_lshl_add_u32 v252, v248, 7, v252\n"
    "s_lshl_b32 s50, s40, 12\n"
    "s_xor_b32 s51, s50, 4096\n"
    "v_add_u32 v208, s50, v252\n"
    "v_add_u32 v209, s51, v252\n"
    "v_xor_b32 v210, 64, v208\n"
    "v_xor_b32 v211, 64, v209\n"
    "s_lshr_b32 s52, s50, 3\n"
    "s_add_u32 s52, s52, 152192\n"
    "v_lshl_add_u32 v224, v248, 4, s52\n"
    "s_lshl_b32 s52, s49, 9\n"
    "s_add_u32 s52, s52, 154240\n"
    "v_lshl_add_u32 v225, v249, 6, s52\n"
    "v_lshlrev_b32 v226, 6, v249\n"
    "v_add_u32 v226, 154288, v226\n"
    "v_lshl_add_u32 v248, v249, 3, v250\n"
    "v_lshrrev_b32 v250, 1, v250\n"
    "v_and_b32 v249, 1, v249\n"
    "v_lshl_add_u32 v250, v249, 1, v250\n"
    "v_lshlrev_b32 v251, 3, v251\n"
    "s_mul_i32 s52, s49, 6\n"
    "s_add_u32 s53, s52, 0\n"
    "s_lshr_b32 s54, s53, 2\n"
    "s_mul_i32 s54, s54, 24576\n"
    "s_add_u32 s54, s54, 49152\n"
    "s_and_b32 s53, s53, 3\n"
    "v_xor_b32 v252, s53, v250\n"
    "v_lshl_add_u32 v252, v252, 5, v251\n"
    "v_lshl_add_u32 v252, v248, 7, v252\n"
    "v_add_u32 v212, s54, v252\n"
    "s_add_u32 s53, s52, 1\n"
    "s_lshr_b32 s54, s53, 2\n"
    "s_mul_i32 s54, s54, 24576\n"
    "s_add_u32 s54, s54, 49152\n"
    "s_and_b32 s53, s53, 3\n"
    "v_xor_b32 v252, s53, v250\n"
    "v_lshl_add_u32 v252, v252, 5, v251\n"
    "v_lshl_add_u32 v252, v248, 7, v252\n"
    "v_add_u32 v213, s54, v252\n"
    "s_add_u32 s53, s52, 2\n"
    "s_lshr_b32 s54, s53, 2\n"
    "s_mul_i32 s54, s54, 24576\n"
    "s_add_u32 s54, s54, 49152\n"
    "s_and_b32 s53, s53, 3\n"
    "v_xor_b32 v252, s53, v250\n"
    "v_lshl_add_u32 v252, v252, 5, v251\n"
    "v_lshl_add_u32 v252, v248, 7, v252\n"
    "v_add_u32 v214, s54, v252\n"
    "s_add_u32 s53, s52, 3\n"
    "s_lshr_b32 s54, s53, 2\n"
    "s_mul_i32 s54, s54, 24576\n"
    "s_add_u32 s54, s54, 49152\n"
    "s_and_b32 s53, s53, 3\n"
    "v_xor_b32 v252, s53, v250\n"
    "v_lshl_add_u32 v252, v252, 5, v251\n"
    "v_lshl_add_u32 v252, v248, 7, v252\n"
    "v_add_u32 v215, s54, v252\n"
    "v_lshl_add_u32 v252, v249, 5, v251\n"
    "v_lshl_add_u32 v252, v248, 6, v252\n"
    "v_add_u32 v216, 122880, v252\n"
    "v_xor_b32 v217, 32, v216\n"
    "s_and_b32 s52, s42, 1\n"
    "s_mul_i32 s53, s49, 3\n"
    "s_add_u32 s70, s53, s52\n"
    "s_xor_b32 s52, s52, 1\n"
    "s_mul_i32 s71, s52, 6\n"
    "s_lshl_b32 s70, s70, 13\n"
    "s_lshl_b32 s71, s71, 13\n"
    "s_lshr_b32 s50, s50, 1\n"
    "s_lshr_b32 s51, s51, 1\n"
    "s_add_u32 s71, s71, s50\n"
    "s_add_u32 s61, s70, s51\n"
    "s_add_u32 s70, s70, s50\n"
    "v_add_u32 v238, s70, v222\n"
    "v_add_u32 v239, s61, v222\n"
    "v_add_u32 v229, s71, v222\n"
    "s_mul_i32 s62, s41, 112\n"
    "s_mul_i32 s52, s42, 7\n"
    "s_add_u32 s60, s62, s52\n"
    "s_lshl_b32 s60, s60, 13\n"
    "s_add_u32 s60, s60, 4718592\n"
    "s_xor_b32 s52, s40, 1\n"
    "s_sub_u32 s52, s52, s40\n"
    "s_lshl_b32 s52, s52, 9\n"
    "v_add_u32 v230, s52, v224\n"
    "s_mul_i32 s50, s46, 7\n"
    "v_lshrrev_b32 v232, 0, v255\n"
    "v_subrev_u32 v250, 7, v232\n"
    "v_subrev_u32 v248, s63, v250\n"
    "v_and_b32 v248, 3, v248\n"
    "v_sub_u32 v249, v250, v248\n"
    "v_cmp_gt_u32 s[64:65], 19, v249\n"
    "v_cmp_eq_u32 vcc, 0, v248\n"
    "v_cmp_ge_u32 s[66:67], 2, v248\n"
    "s_nop 1\n"
    "v_cndmask_b32 v249, 0, -1, s[66:67]\n"
    "v_cndmask_b32 v249, v249, 2, vcc\n"
    "v_cndmask_b32 v249, 0, v249, s[64:65]\n"
    "v_add_u32 v250, v250, v249\n"
    "v_add_u32 v250, 7, v250\n"
    "v_cmp_gt_u32 vcc, 7, v232\n"
    "s_nop 1\n"
    "v_cndmask_b32 v232, v250, v232, vcc\n"
    "v_mov_b32 v248, s48\n"
    "v_lshl_add_u32 v232, v232, 1, v248\n"
    "s_cmp_eq_u32 s46, 1\n"
    "s_cselect_b32 s64, 3129426704, 0\n"
    "s_cselect_b32 s65, 14456402, 0\n"
    "s_cselect_b32 s66, 0, 3129431073\n"
    "s_cselect_b32 s67, 0, 14454883\n"
    "s_or_b64 s[64:65], s[64:65], s[66:67]\n"
    "v_subrev_u32 v248, 7, v232\n"
    "v_lshlrev_b32 v249, 2, v248\n"
    "v_lshrrev_b64 v[234:235], v249, s[64:65]\n"
    "v_and_b32 v249, 15, v234\n"
    "v_add_u32 v249, 7, v249\n"
    "v_cmp_gt_u32 vcc, 14, v248\n"
    "s_nop 1\n"
    "v_cndmask_b32 v232, v232, v249, vcc\n"
    "v_add_u32 v248, s50, v232\n"
    "v_subrev_u32 v249, s50, v232\n"
    "v_cmp_gt_u32 vcc, 7, v249\n"
    "s_nop 1\n"
    "v_cndmask_b32 v249, v232, v249, vcc\n"
    "v_cmp_gt_u32 vcc, 7, v232\n"
    "s_nop 1\n"
    "v_cndmask_b32 v232, v249, v248, vcc\n"
    "v_lshrrev_b32 v248, 4, v255\n"
    "v_lshlrev_b32 v248, 1, v248\n"
    "v_and_b32 v249, 7, v255\n"
    "v_xor_b32 v248, v248, v249\n"
    "v_lshlrev_b32 v248, 4, v248\n"
    "v_lshrrev_b32 v249, 3, v255\n"
    "v_lshl_add_u32 v248, v249, 11, v248\n"
    "s_mul_i32 s50, s45, 3670016\n"
    "s_lshl_b32 s51, s46, 8\n"
    "s_add_u32 s50, s50, s51\n"
    "v_add_u32 v234, s50, v248\n"
    "v_add_u32 v235, 15360, v234\n"
    "v_add_u32 v236, 30720, v234\n"
    "v_add_u32 v237, 46080, v234\n"
    "s_mov_b32 s32, s18\n"
    "s_and_b32 s33, s19, 0xffff\n"
    "s_mov_b32 s34, 7340032\n"
    "s_mov_b32 s35, 0x20000\n"
    "s_mov_b32 s28, s4\n"
    "s_and_b32 s29, s5, 0xffff\n"
    "s_mov_b32 s30, 4194304\n"
    "s_mov_b32 s31, 0x20000\n"
    "v_mov_b32 v48, 0\n"
    "v_mov_b32 v49, 0\n"
    "v_mov_b32 v50, 0\n"
    "v_mov_b32 v51, 0\n"
    "v_mov_b32 v52, 0\n"
    "v_mov_b32 v53, 0\n"
    "v_mov_b32 v54, 0\n"
    "v_mov_b32 v55, 0\n"
    "v_mov_b32 v64, 0\n"
    "v_mov_b32 v65, 0\n"
    "v_mov_b32 v66, 0\n"
    "v_mov_b32 v67, 0\n"
    "v_mov_b32 v68, 0\n"
    "v_mov_b32 v69, 0\n"
    "v_mov_b32 v70, 0\n"
    "v_mov_b32 v71, 0\n"
    "v_mov_b32 v72, 0\n"
    "v_mov_b32 v73, 0\n"
    "v_mov_b32 v74, 0\n"
    "v_mov_b32 v75, 0\n"
    "v_mov_b32 v76, 0\n"
    "v_mov_b32 v77, 0\n"
    "v_mov_b32 v78, 0\n"
    "v_mov_b32 v79, 0\n"
    "v_mov_b32 v80, 0\n"
    "v_mov_b32 v81, 0\n"
    "v_mov_b32 v82, 0\n"
    "v_mov_b32 v83, 0\n"
    "v_mov_b32 v84, 0\n"
    "v_mov_b32 v85, 0\n"
    "v_mov_b32 v86, 0\n"
    "v_mov_b32 v87, 0\n"
    "v_mov_b32 v88, 0\n"
    "v_mov_b32 v89, 0\n"
    "v_mov_b32 v90, 0\n"
    "v_mov_b32 v91, 0\n"
    "v_mov_b32 v92, 0\n"
    "v_mov_b32 v93, 0\n"
    "v_mov_b32 v94, 0\n"
    "v_mov_b32 v95, 0\n"
    "v_mov_b32 v96, 0\n"
    "v_mov_b32 v97, 0\n"
    "v_mov_b32 v98, 0\n"
    "v_mov_b32 v99, 0\n"
    "v_mov_b32 v100, 0\n"
    "v_mov_b32 v101, 0\n"
    "v_mov_b32 v102, 0\n"
    "v_mov_b32 v103, 0\n"
    "v_mov_b32 v104, 0\n"
    "v_mov_b32 v105, 0\n"
    "v_mov_b32 v106, 0\n"
    "v_mov_b32 v107, 0\n"
    "v_mov_b32 v108, 0\n"
    "v_mov_b32 v109, 0\n"
    "v_mov_b32 v110, 0\n"
    "v_mov_b32 v111, 0\n"
    "s_waitcnt vmcnt(1)\n"
    "v_lshlrev_b32 v112, 16, v160\n"
    "v_and_b32 v113, 0xffff0000, v160\n"
    "v_lshlrev_b32 v114, 16, v161\n"
    "v_and_b32 v115, 0xffff0000, v161\n"
    "v_lshlrev_b32 v116, 16, v162\n"
    "v_and_b32 v117, 0xffff0000, v162\n"
    "v_lshlrev_b32 v118, 16, v163\n"
    "v_and_b32 v119, 0xffff0000, v163\n"
    "v_lshlrev_b32 v120, 16, v164\n"
    "v_and_b32 v121, 0xffff0000, v164\n"
    "v_lshlrev_b32 v122, 16, v165\n"
    "v_and_b32 v123, 0xffff0000, v165\n"
    "v_lshlrev_b32 v124, 16, v166\n"
    "v_and_b32 v125, 0xffff0000, v166\n"
    "v_lshlrev_b32 v126, 16, v167\n"
    "v_and_b32 v127, 0xffff0000, v167\n"
    "v_lshlrev_b32 v128, 16, v168\n"
    "v_and_b32 v129, 0xffff0000, v168\n"
    "v_lshlrev_b32 v130, 16, v169\n"
    "v_and_b32 v131, 0xffff0000, v169\n"
    "v_lshlrev_b32 v132, 16, v170\n"
    "v_and_b32 v133, 0xffff0000, v170\n"
    "v_lshlrev_b32 v134, 16, v171\n"
    "v_and_b32 v135, 0xffff0000, v171\n"
    "v_lshlrev_b32 v136, 16, v172\n"
    "v_and_b32 v137, 0xffff0000, v172\n"
    "v_lshlrev_b32 v138, 16, v173\n"
    "v_and_b32 v139, 0xffff0000, v173\n"
    "v_lshlrev_b32 v140, 16, v174\n"
    "v_and_b32 v141, 0xffff0000, v174\n"
    "v_lshlrev_b32 v142, 16, v175\n"
    "v_and_b32 v143, 0xffff0000, v175\n"
    "v_add_f32 v144, v113, v112\n"
    "v_add_f32 v145, v129, v128\n"
    "v_add_f32 v144, v114, v144\n"
    "v_add_f32 v145, v130, v145\n"
    "v_add_f32 v144, v115, v144\n"
    "v_add_f32 v145, v131, v145\n"
    "v_add_f32 v144, v116, v144\n"
    "v_add_f32 v145, v132, v145\n"
    "v_add_f32 v144, v117, v144\n"
    "v_add_f32 v145, v133, v145\n"
    "v_add_f32 v144, v118, v144\n"
    "v_add_f32 v145, v134, v145\n"
    "v_add_f32 v144, v119, v144\n"
    "v_add_f32 v145, v135, v145\n"
    "v_add_f32 v144, v120, v144\n"
    "v_add_f32 v145, v136, v145\n"
    "v_add_f32 v144, v121, v144\n"
    "v_add_f32 v145, v137, v145\n"
    "v_add_f32 v144, v122, v144\n"
    "v_add_f32 v145, v138, v145\n"
    "v_add_f32 v144, v123, v144\n"
    "v_add_f32 v145, v139, v145\n"
    "v_add_f32 v144, v124, v144\n"
    "v_add_f32 v145, v140, v145\n"
    "v_add_f32 v144, v125, v144\n"
    "v_add_f32 v145, v141, v145\n"
    "v_add_f32 v144, v126, v144\n"
    "v_add_f32 v145, v142, v145\n"
    "v_add_f32 v144, v127, v144\n"
    "v_add_f32 v145, v143, v145\n"
    "s_nop 0\n"
    "v_add_f32_dpp v144, v144, v144 row_shr:1 row_mask:0xf bank_mask:0xf bound_ctrl:0\n"
    "v_add_f32_dpp v145, v145, v145 row_shr:1 row_mask:0xf bank_mask:0xf bound_ctrl:0\n"
    "s_nop 0\n"
    "v_add_f32_dpp v144, v144, v144 row_shr:2 row_mask:0xf bank_mask:0xf bound_ctrl:0\n"
    "v_add_f32_dpp v145, v145, v145 row_shr:2 row_mask:0xf bank_mask:0xf bound_ctrl:0\n"
    "s_nop 0\n"
    "v_add_f32_dpp v144, v144, v144 row_shr:4 row_mask:0xf bank_mask:0xf bound_ctrl:0\n"
    "v_add_f32_dpp v145, v145, v145 row_shr:4 row_mask:0xf bank_mask:0xf bound_ctrl:0\n"
    "s_nop 0\n"
    "v_add_f32_dpp v144, v144, v144 row_shr:8 row_mask:0xf bank_mask:0xf bound_ctrl:0\n"
    "v_add_f32_dpp v145, v145, v145 row_shr:8 row_mask:0xf bank_mask:0xf bound_ctrl:0\n"
    "s_nop 0\n"
    "v_add_f32_dpp v144, v144, v144 row_bcast:15 row_mask:0xa bank_mask:0xf\n"
    "v_add_f32_dpp v145, v145, v145 row_bcast:15 row_mask:0xa bank_mask:0xf\n"
    "s_nop 0\n"
    "v_add_f32_dpp v144, v144, v144 row_bcast:31 row_mask:0xc bank_mask:0xf\n"
    "v_add_f32_dpp v145, v145, v145 row_bcast:31 row_mask:0xc bank_mask:0xf\n"
    "v_mul_f32 v144, 0.0009765625, v144\n"
    "v_mul_f32 v145, 0.0009765625, v145\n"
    "v_readlane_b32 s56, v144, 63\n"
    "v_readlane_b32 s57, v145, 63\n"
    "s_nop 0\n"
    "v_subrev_f32 v112, s56, v112\n"
    "v_subrev_f32 v128, s57, v128\n"
    "v_subrev_f32 v113, s56, v113\n"
    "v_subrev_f32 v129, s57, v129\n"
    "v_subrev_f32 v114, s56, v114\n"
    "v_subrev_f32 v130, s57, v130\n"
    "v_subrev_f32 v115, s56, v115\n"
    "v_subrev_f32 v131, s57, v131\n"
    "v_subrev_f32 v116, s56, v116\n"
    "v_subrev_f32 v132, s57, v132\n"
    "v_subrev_f32 v117, s56, v117\n"
    "v_subrev_f32 v133, s57, v133\n"
    "v_subrev_f32 v118, s56, v118\n"
    "v_subrev_f32 v134, s57, v134\n"
    "v_subrev_f32 v119, s56, v119\n"
    "v_subrev_f32 v135, s57, v135\n"
    "v_subrev_f32 v120, s56, v120\n"
    "v_subrev_f32 v136, s57, v136\n"
    "v_subrev_f32 v121, s56, v121\n"
    "v_subrev_f32 v137, s57, v137\n"
    "v_subrev_f32 v122, s56, v122\n"
    "v_subrev_f32 v138, s57, v138\n"
    "v_subrev_f32 v123, s56, v123\n"
    "v_subrev_f32 v139, s57, v139\n"
    "v_subrev_f32 v124, s56, v124\n"
    "v_subrev_f32 v140, s57, v140\n"
    "v_subrev_f32 v125, s56, v125\n"
    "v_subrev_f32 v141, s57, v141\n"
    "v_subrev_f32 v126, s56, v126\n"
    "v_subrev_f32 v142, s57, v142\n"
    "v_subrev_f32 v127, s56, v127\n"
    "v_subrev_f32 v143, s57, v143\n"
    "v_mul_f32 v144, v112, v112\n"
    "v_mul_f32 v145, v128, v128\n"
    "v_fmac_f32 v144, v113, v113\n"
    "v_fmac_f32 v145, v129, v129\n"
    "v_fmac_f32 v144, v114, v114\n"
    "v_fmac_f32 v145, v130, v130\n"
    "v_fmac_f32 v144, v115, v115\n"
    "v_fmac_f32 v145, v131, v131\n"
    "v_fmac_f32 v144, v116, v116\n"
    "v_fmac_f32 v145, v132, v132\n"
    "v_fmac_f32 v144, v117, v117\n"
    "v_fmac_f32 v145, v133, v133\n"
    "v_fmac_f32 v144, v118, v118\n"
    "v_fmac_f32 v145, v134, v134\n"
    "v_fmac_f32 v144, v119, v119\n"
    "v_fmac_f32 v145, v135, v135\n"
    "v_fmac_f32 v144, v120, v120\n"
    "v_fmac_f32 v145, v136, v136\n"
    "v_fmac_f32 v144, v121, v121\n"
    "v_fmac_f32 v145, v137, v137\n"
    "v_fmac_f32 v144, v122, v122\n"
    "v_fmac_f32 v145, v138, v138\n"
    "v_fmac_f32 v144, v123, v123\n"
    "v_fmac_f32 v145, v139, v139\n"
    "v_fmac_f32 v144, v124, v124\n"
    "v_fmac_f32 v145, v140, v140\n"
    "v_fmac_f32 v144, v125, v125\n"
    "v_fmac_f32 v145, v141, v141\n"
    "v_fmac_f32 v144, v126, v126\n"
    "v_fmac_f32 v145, v142, v142\n"
    "v_fmac_f32 v144, v127, v127\n"
    "v_fmac_f32 v145, v143, v143\n"
    "s_nop 0\n"
    "v_add_f32_dpp v144, v144, v144 row_shr:1 row_mask:0xf bank_mask:0xf bound_ctrl:0\n"
    "v_add_f32_dpp v145, v145, v145 row_shr:1 row_mask:0xf bank_mask:0xf bound_ctrl:0\n"
    "s_nop 0\n"
    "v_add_f32_dpp v144, v144, v144 row_shr:2 row_mask:0xf bank_mask:0xf bound_ctrl:0\n"
    "v_add_f32_dpp v145, v145, v145 row_shr:2 row_mask:0xf bank_mask:0xf bound_ctrl:0\n"
    "s_nop 0\n"
    "v_add_f32_dpp v144, v144, v144 row_shr:4 row_mask:0xf bank_mask:0xf bound_ctrl:0\n"
    "v_add_f32_dpp v145, v145, v145 row_shr:4 row_mask:0xf bank_mask:0xf bound_ctrl:0\n"
    "s_nop 0\n"
    "v_add_f32_dpp v144, v144, v144 row_shr:8 row_mask:0xf bank_mask:0xf bound_ctrl:0\n"
    "v_add_f32_dpp v145, v145, v145 row_shr:8 row_mask:0xf bank_mask:0xf bound_ctrl:0\n"
    "s_nop 0\n"
    "v_add_f32_dpp v144, v144, v144 row_bcast:15 row_mask:0xa bank_mask:0xf\n"
    "v_add_f32_dpp v145, v145, v145 row_bcast:15 row_mask:0xa bank_mask:0xf\n"
    "s_nop 0\n"
    "v_add_f32_dpp v144, v144, v144 row_bcast:31 row_mask:0xc bank_mask:0xf\n"
    "v_add_f32_dpp v145, v145, v145 row_bcast:31 row_mask:0xc bank_mask:0xf\n"
    "v_readlane_b32 s54, v144, 63\n"
    "v_readlane_b32 s55, v145, 63\n"
    "v_mov_b32 v112, s56\n"
    "v_mov_b32 v113, s54\n"
    "v_writelane_b32 v112, s57, 2\n"
    "v_writelane_b32 v113, s55, 2\n"
    "v_mul_f32 v113, 0.0009765625, v113\n"
    "v_cvt_pk_bf16_f32 v114, -v112, v113\n"
    "v_lshlrev_b32 v112, 16, v114\n"
    "v_and_b32 v113, 0xffff0000, v114\n"
    "v_add_f32 v113, 0x3727c5ac, v113\n"
    "v_cvt_pk_bf16_f32 v113, v113, v113\n"
    "v_lshlrev_b32 v113, 16, v113\n"
    "v_sqrt_f32 v113, v113\n"
    "s_nop 0\n"
    "v_cvt_pk_bf16_f32 v113, v113, v113\n"
    "v_lshlrev_b32 v113, 16, v113\n"
    "v_rcp_f32 v114, v113\n"
    "s_nop 0\n"
    "v_mul_f32 v115, 0xbfb8aa3b, v114\n"
    "s_lshl2_add_u32 s53, s2, s40\n"
    "s_lshl_b32 s53, s53, 6\n"
    "s_add_u32 s53, s53, 98304\n"
    "s_mov_b64 exec, 15\n"
    "buffer_store_dwordx4 v[112:115], v222, s[24:27], s53 offen sc1\n"
    "s_mov_b64 exec, -1\n"
    "s_or_b32 s50, s2, s40\n"
    "s_cbranch_scc1 Lz26_%=\n"
    "v_lshlrev_b32 v249, 7, v255\n"
    "buffer_store_dword v223, v249, s[24:27], 0 offen offset:256 sc1\n"
    "Lz26_%=:\n"
    "s_waitcnt lgkmcnt(0)\n"
    "s_mov_b32 s58, 5\n"
    "s_barrier\n"
    "ds_read_b128 v[144:147], v209 offset:2048\n"
    "ds_read_b128 v[148:151], v211 offset:2048\n"
    "ds_read_b64_tr_b16 v[112:113], v212 offset:0\n"
    "ds_read_b64_tr_b16 v[114:115], v212 offset:512\n"
    "ds_read_b64_tr_b16 v[116:117], v212 offset:4096\n"
    "ds_read_b64_tr_b16 v[118:119], v212 offset:4608\n"
    "ds_read_b128 v[136:139], v209 offset:0\n"
    "ds_read_b128 v[140:143], v211 offset:0\n"
    "ds_read_b64_tr_b16 v[120:121], v213 offset:0\n"
    "ds_read_b64_tr_b16 v[122:123], v213 offset:512\n"
    "ds_read_b64_tr_b16 v[124:125], v213 offset:4096\n"
    "ds_read_b64_tr_b16 v[126:127], v213 offset:4608\n"
    "ds_read_b64_tr_b16 v[128:129], v214 offset:0\n"
    "ds_read_b64_tr_b16 v[130:131], v214 offset:512\n"
    "ds_read_b64_tr_b16 v[132:133], v214 offset:4096\n"
    "ds_read_b64_tr_b16 v[134:135], v214 offset:4608\n"
    "ds_read_b64_tr_b16 v[152:153], v213 offset:24576\n"
    "ds_read_b64_tr_b16 v[154:155], v213 offset:25088\n"
    "ds_read_b64_tr_b16 v[156:157], v213 offset:28672\n"
    "ds_read_b64_tr_b16 v[158:159], v213 offset:29184\n"
    "s_waitcnt lgkmcnt(15)\n"
    "v_mfma_f32_16x16x32_bf16 v[12:15], v[112:115], v[144:147], 0\n"
    "ds_read_b128 v[200:203], v208 offset:2048\n"
    "s_waitcnt lgkmcnt(15)\n"
    "v_mfma_f32_16x16x32_bf16 v[12:15], v[116:119], v[148:151], v[12:15]\n"
    "ds_read_b128 v[204:207], v210 offset:2048\n"
    "s_waitcnt lgkmcnt(14)\n"
    "v_mfma_f32_16x16x32_bf16 v[8:11], v[116:119], v[140:143], 0\n"
    "ds_read_b128 v[192:195], v208 offset:0\n"
    "v_mfma_f32_16x16x32_bf16 v[8:11], v[112:115], v[136:139], v[8:11]\n"
    "ds_read_b128 v[196:199], v210 offset:0\n"
    "s_waitcnt lgkmcnt(14)\n"
    "v_mfma_f32_16x16x32_bf16 v[24:27], v[120:123], v[136:139], 0\n"
    "ds_read_b64_tr_b16 v[168:169], v215 offset:0\n"
    "s_waitcnt lgkmcnt(5)\n"
    "v_mfma_f32_16x16x32_bf16 v[24:27], v[124:127], v[140:143], v[24:27]\n"
    "ds_read_b64_tr_b16 v[170:171], v215 offset:512\n"
    "v_mfma_f32_16x16x32_bf16 v[28:31], v[124:127], v[148:151], 0\n"
    "ds_read_b64_tr_b16 v[172:173], v215 offset:4096\n"
    "v_mfma_f32_16x16x32_bf16 v[28:31], v[120:123], v[144:147], v[28:31]\n"
    "ds_read_b64_tr_b16 v[174:175], v215 offset:4608\n"
    "v_mfma_f32_16x16x32_bf16 v[44:47], v[128:131], v[144:147], 0\n"
    "ds_read_b64_tr_b16 v[160:161], v212 offset:24576\n"
    "v_mfma_f32_16x16x32_bf16 v[44:47], v[132:135], v[148:151], v[44:47]\n"
    "ds_read_b64_tr_b16 v[162:163], v212 offset:25088\n"
    "v_mfma_f32_16x16x32_bf16 v[40:43], v[132:135], v[140:143], 0\n"
    "ds_read_b64_tr_b16 v[164:165], v212 offset:28672\n"
    "v_mfma_f32_16x16x32_bf16 v[40:43], v[128:131], v[136:139], v[40:43]\n"
    "ds_read_b64_tr_b16 v[166:167], v212 offset:29184\n"
    "s_waitcnt lgkmcnt(5)\n"
    "v_mfma_f32_16x16x32_bf16 v[36:39], v[128:131], v[200:203], 0\n"
    "ds_read_b64_tr_b16 v[176:177], v216 offset:0\n"
    "v_mfma_f32_16x16x32_bf16 v[36:39], v[132:135], v[204:207], v[36:39]\n"
    "ds_read_b64_tr_b16 v[178:179], v216 offset:256\n"
    "v_mfma_f32_16x16x32_bf16 v[32:35], v[132:135], v[196:199], 0\n"
    "ds_read_b64_tr_b16 v[180:181], v216 offset:2048\n"
    "v_mfma_f32_16x16x32_bf16 v[32:35], v[128:131], v[192:195], v[32:35]\n"
    "ds_read_b64_tr_b16 v[182:183], v216 offset:2304\n"
    "v_mfma_f32_16x16x32_bf16 v[16:19], v[120:123], v[192:195], 0\n"
    "ds_read_b64_tr_b16 v[184:185], v217 offset:0\n"
    "v_mfma_f32_16x16x32_bf16 v[16:19], v[124:127], v[196:199], v[16:19]\n"
    "ds_read_b64_tr_b16 v[186:187], v217 offset:256\n"
    "v_mfma_f32_16x16x32_bf16 v[20:23], v[124:127], v[204:207], 0\n"
    "ds_read_b64_tr_b16 v[188:189], v217 offset:2048\n"
    "v_mfma_f32_16x16x32_bf16 v[20:23], v[120:123], v[200:203], v[20:23]\n"
    "ds_read_b64_tr_b16 v[190:191], v217 offset:2304\n"
    "v_mfma_f32_16x16x32_bf16 v[4:7], v[112:115], v[200:203], 0\n"
    "v_mfma_f32_16x16x32_bf16 v[4:7], v[116:119], v[204:207], v[4:7]\n"
    "v_mfma_f32_16x16x32_bf16 v[0:3], v[116:119], v[196:199], 0\n"
    "v_mfma_f32_16x16x32_bf16 v[0:3], v[112:115], v[192:195], v[0:3]\n"
    "v_mfma_f32_16x16x32_bf16 v[56:59], v[168:171], v[136:139], 0\n"
    "s_waitcnt lgkmcnt(0)\n"
    "v_mfma_f32_16x16x32_bf16 v[56:59], v[172:175], v[140:143], v[56:59]\n"
    "v_mfma_f32_16x16x32_bf16 v[60:63], v[172:175], v[148:151], 0\n"
    "v_mfma_f32_16x16x32_bf16 v[60:63], v[168:171], v[144:147], v[60:63]\n"
    "s_waitcnt lgkmcnt(0)\n"
    "Lg1_%=:\n"
    "s_barrier\n"
    "v_mfma_f32_16x16x32_bf16 v[76:79], v[160:163], v[144:147], v[76:79]\n"
    "ds_read_b64_tr_b16 v[112:113], v212 offset:8192\n"
    "v_mfma_f32_16x16x32_bf16 v[76:79], v[164:167], v[148:151], v[76:79]\n"
    "ds_read_b64_tr_b16 v[114:115], v212 offset:8704\n"
    "v_mfma_f32_16x16x32_bf16 v[72:75], v[164:167], v[140:143], v[72:75]\n"
    "ds_read_b64_tr_b16 v[116:117], v212 offset:12288\n"
    "v_mfma_f32_16x16x32_bf16 v[72:75], v[160:163], v[136:139], v[72:75]\n"
    "ds_read_b64_tr_b16 v[118:119], v212 offset:12800\n"
    "v_mfma_f32_16x16x32_bf16 v[88:91], v[152:155], v[136:139], v[88:91]\n"
    "ds_read_b64_tr_b16 v[120:121], v213 offset:8192\n"
    "v_mfma_f32_16x16x32_bf16 v[88:91], v[156:159], v[140:143], v[88:91]\n"
    "ds_read_b64_tr_b16 v[122:123], v213 offset:8704\n"
    "v_mfma_f32_16x16x32_bf16 v[92:95], v[156:159], v[148:151], v[92:95]\n"
    "ds_read_b64_tr_b16 v[124:125], v213 offset:12288\n"
    "v_mfma_f32_16x16x32_bf16 v[92:95], v[152:155], v[144:147], v[92:95]\n"
    "ds_read_b64_tr_b16 v[126:127], v213 offset:12800\n"
    "v_mfma_f32_16x16x32_bf16 v[84:87], v[152:155], v[200:203], v[84:87]\n"
    "ds_read_b128 v[144:147], v209 offset:18432\n"
    "v_mfma_f32_16x16x32_bf16 v[84:87], v[156:159], v[204:207], v[84:87]\n"
    "ds_read_b128 v[148:151], v211 offset:18432\n"
    "v_mfma_f32_16x16x32_bf16 v[80:83], v[156:159], v[196:199], v[80:83]\n"
    "ds_read_b128 v[136:139], v209 offset:16384\n"
    "v_mfma_f32_16x16x32_bf16 v[80:83], v[152:155], v[192:195], v[80:83]\n"
    "ds_read_b128 v[140:143], v211 offset:16384\n"
    "v_mfma_f32_16x16x32_bf16 v[64:67], v[160:163], v[192:195], v[64:67]\n"
    "ds_read_b64_tr_b16 v[128:129], v214 offset:8192\n"
    "v_mfma_f32_16x16x32_bf16 v[64:67], v[164:167], v[196:199], v[64:67]\n"
    "ds_read_b64_tr_b16 v[130:131], v214 offset:8704\n"
    "v_mfma_f32_16x16x32_bf16 v[68:71], v[164:167], v[204:207], v[68:71]\n"
    "ds_read_b64_tr_b16 v[132:133], v214 offset:12288\n"
    "v_mfma_f32_16x16x32_bf16 v[68:71], v[160:163], v[200:203], v[68:71]\n"
    "ds_read_b64_tr_b16 v[134:135], v214 offset:12800\n"
    "v_mfma_f32_16x16x32_bf16 v[52:55], v[168:171], v[200:203], v[52:55]\n"
    "ds_read_b64_tr_b16 v[152:153], v213 offset:32768\n"
    "v_mfma_f32_16x16x32_bf16 v[52:55], v[172:175], v[204:207], v[52:55]\n"
    "ds_read_b64_tr_b16 v[154:155], v213 offset:33280\n"
    "v_mfma_f32_16x16x32_bf16 v[48:51], v[172:175], v[196:199], v[48:51]\n"
    "ds_read_b64_tr_b16 v[156:157], v213 offset:36864\n"
    "v_mfma_f32_16x16x32_bf16 v[48:51], v[168:171], v[192:195], v[48:51]\n"
    "ds_read_b64_tr_b16 v[158:159], v213 offset:37376\n"
    "v_mfma_f32_16x16x32_bf16 v[96:99], v[176:179], v[192:195], v[96:99]\n"
    "v_mfma_f32_16x16x32_bf16 v[96:99], v[180:183], v[196:199], v[96:99]\n"
    "v_mfma_f32_16x16x32_bf16 v[100:103], v[180:183], v[204:207], v[100:103]\n"
    "v_mfma_f32_16x16x32_bf16 v[100:103], v[176:179], v[200:203], v[100:103]\n"
    "v_mfma_f32_16x16x32_bf16 v[108:111], v[184:187], v[200:203], v[108:111]\n"
    "v_mfma_f32_16x16x32_bf16 v[108:111], v[188:191], v[204:207], v[108:111]\n"
    "v_mfma_f32_16x16x32_bf16 v[104:107], v[188:191], v[196:199], v[104:107]\n"
    "v_mfma_f32_16x16x32_bf16 v[104:107], v[184:187], v[192:195], v[104:107]\n"
    "s_waitcnt lgkmcnt(0)\n"
    "v_mfma_f32_16x16x32_bf16 v[12:15], v[112:115], v[144:147], v[12:15]\n"
    "ds_read_b128 v[200:203], v208 offset:18432\n"
    "v_mfma_f32_16x16x32_bf16 v[12:15], v[116:119], v[148:151], v[12:15]\n"
    "ds_read_b128 v[204:207], v210 offset:18432\n"
    "v_mfma_f32_16x16x32_bf16 v[8:11], v[116:119], v[140:143], v[8:11]\n"
    "ds_read_b128 v[192:195], v208 offset:16384\n"
    "v_mfma_f32_16x16x32_bf16 v[8:11], v[112:115], v[136:139], v[8:11]\n"
    "ds_read_b128 v[196:199], v210 offset:16384\n"
    "v_mfma_f32_16x16x32_bf16 v[24:27], v[120:123], v[136:139], v[24:27]\n"
    "ds_read_b64_tr_b16 v[168:169], v215 offset:8192\n"
    "v_mfma_f32_16x16x32_bf16 v[24:27], v[124:127], v[140:143], v[24:27]\n"
    "ds_read_b64_tr_b16 v[170:171], v215 offset:8704\n"
    "v_mfma_f32_16x16x32_bf16 v[28:31], v[124:127], v[148:151], v[28:31]\n"
    "ds_read_b64_tr_b16 v[172:173], v215 offset:12288\n"
    "v_mfma_f32_16x16x32_bf16 v[28:31], v[120:123], v[144:147], v[28:31]\n"
    "ds_read_b64_tr_b16 v[174:175], v215 offset:12800\n"
    "v_mfma_f32_16x16x32_bf16 v[44:47], v[128:131], v[144:147], v[44:47]\n"
    "ds_read_b64_tr_b16 v[160:161], v212 offset:32768\n"
    "v_mfma_f32_16x16x32_bf16 v[44:47], v[132:135], v[148:151], v[44:47]\n"
    "ds_read_b64_tr_b16 v[162:163], v212 offset:33280\n"
    "v_mfma_f32_16x16x32_bf16 v[40:43], v[132:135], v[140:143], v[40:43]\n"
    "ds_read_b64_tr_b16 v[164:165], v212 offset:36864\n"
    "v_mfma_f32_16x16x32_bf16 v[40:43], v[128:131], v[136:139], v[40:43]\n"
    "ds_read_b64_tr_b16 v[166:167], v212 offset:37376\n"
    "s_waitcnt lgkmcnt(5)\n"
    "v_mfma_f32_16x16x32_bf16 v[36:39], v[128:131], v[200:203], v[36:39]\n"
    "ds_read_b64_tr_b16 v[176:177], v216 offset:4096\n"
    "v_mfma_f32_16x16x32_bf16 v[36:39], v[132:135], v[204:207], v[36:39]\n"
    "ds_read_b64_tr_b16 v[178:179], v216 offset:4352\n"
    "v_mfma_f32_16x16x32_bf16 v[32:35], v[132:135], v[196:199], v[32:35]\n"
    "ds_read_b64_tr_b16 v[180:181], v216 offset:6144\n"
    "v_mfma_f32_16x16x32_bf16 v[32:35], v[128:131], v[192:195], v[32:35]\n"
    "ds_read_b64_tr_b16 v[182:183], v216 offset:6400\n"
    "v_mfma_f32_16x16x32_bf16 v[16:19], v[120:123], v[192:195], v[16:19]\n"
    "ds_read_b64_tr_b16 v[184:185], v217 offset:4096\n"
    "v_mfma_f32_16x16x32_bf16 v[16:19], v[124:127], v[196:199], v[16:19]\n"
    "ds_read_b64_tr_b16 v[186:187], v217 offset:4352\n"
    "v_mfma_f32_16x16x32_bf16 v[20:23], v[124:127], v[204:207], v[20:23]\n"
    "ds_read_b64_tr_b16 v[188:189], v217 offset:6144\n"
    "v_mfma_f32_16x16x32_bf16 v[20:23], v[120:123], v[200:203], v[20:23]\n"
    "ds_read_b64_tr_b16 v[190:191], v217 offset:6400\n"
    "v_mfma_f32_16x16x32_bf16 v[4:7], v[112:115], v[200:203], v[4:7]\n"
    "v_mfma_f32_16x16x32_bf16 v[4:7], v[116:119], v[204:207], v[4:7]\n"
    "v_mfma_f32_16x16x32_bf16 v[0:3], v[116:119], v[196:199], v[0:3]\n"
    "v_mfma_f32_16x16x32_bf16 v[0:3], v[112:115], v[192:195], v[0:3]\n"
    "v_mfma_f32_16x16x32_bf16 v[56:59], v[168:171], v[136:139], v[56:59]\n"
    "s_waitcnt lgkmcnt(0)\n"
    "v_mfma_f32_16x16x32_bf16 v[56:59], v[172:175], v[140:143], v[56:59]\n"
    "v_mfma_f32_16x16x32_bf16 v[60:63], v[172:175], v[148:151], v[60:63]\n"
    "v_mfma_f32_16x16x32_bf16 v[60:63], v[168:171], v[144:147], v[60:63]\n"
    "s_waitcnt lgkmcnt(0)\n"
    "s_barrier\n"
    "v_mfma_f32_16x16x32_bf16 v[76:79], v[160:163], v[144:147], v[76:79]\n"
    "ds_read_b64_tr_b16 v[112:113], v212 offset:16384\n"
    "v_mfma_f32_16x16x32_bf16 v[76:79], v[164:167], v[148:151], v[76:79]\n"
    "ds_read_b64_tr_b16 v[114:115], v212 offset:16896\n"
    "v_mfma_f32_16x16x32_bf16 v[72:75], v[164:167], v[140:143], v[72:75]\n"
    "ds_read_b64_tr_b16 v[116:117], v212 offset:20480\n"
    "v_mfma_f32_16x16x32_bf16 v[72:75], v[160:163], v[136:139], v[72:75]\n"
    "ds_read_b64_tr_b16 v[118:119], v212 offset:20992\n"
    "v_mfma_f32_16x16x32_bf16 v[88:91], v[152:155], v[136:139], v[88:91]\n"
    "ds_read_b64_tr_b16 v[120:121], v213 offset:16384\n"
    "v_mfma_f32_16x16x32_bf16 v[88:91], v[156:159], v[140:143], v[88:91]\n"
    "ds_read_b64_tr_b16 v[122:123], v213 offset:16896\n"
    "v_mfma_f32_16x16x32_bf16 v[92:95], v[156:159], v[148:151], v[92:95]\n"
    "ds_read_b64_tr_b16 v[124:125], v213 offset:20480\n"
    "v_mfma_f32_16x16x32_bf16 v[92:95], v[152:155], v[144:147], v[92:95]\n"
    "ds_read_b64_tr_b16 v[126:127], v213 offset:20992\n"
    "v_mfma_f32_16x16x32_bf16 v[84:87], v[152:155], v[200:203], v[84:87]\n"
    "ds_read_b128 v[144:147], v209 offset:34816\n"
    "v_mfma_f32_16x16x32_bf16 v[84:87], v[156:159], v[204:207], v[84:87]\n"
    "ds_read_b128 v[148:151], v211 offset:34816\n"
    "v_mfma_f32_16x16x32_bf16 v[80:83], v[156:159], v[196:199], v[80:83]\n"
    "ds_read_b128 v[136:139], v209 offset:32768\n"
    "v_mfma_f32_16x16x32_bf16 v[80:83], v[152:155], v[192:195], v[80:83]\n"
    "ds_read_b128 v[140:143], v211 offset:32768\n"
    "v_mfma_f32_16x16x32_bf16 v[64:67], v[160:163], v[192:195], v[64:67]\n"
    "ds_read_b64_tr_b16 v[128:129], v214 offset:16384\n"
    "v_mfma_f32_16x16x32_bf16 v[64:67], v[164:167], v[196:199], v[64:67]\n"
    "ds_read_b64_tr_b16 v[130:131], v214 offset:16896\n"
    "v_mfma_f32_16x16x32_bf16 v[68:71], v[164:167], v[204:207], v[68:71]\n"
    "ds_read_b64_tr_b16 v[132:133], v214 offset:20480\n"
    "v_mfma_f32_16x16x32_bf16 v[68:71], v[160:163], v[200:203], v[68:71]\n"
    "ds_read_b64_tr_b16 v[134:135], v214 offset:20992\n"
    "v_mfma_f32_16x16x32_bf16 v[52:55], v[168:171], v[200:203], v[52:55]\n"
    "ds_read_b64_tr_b16 v[152:153], v213 offset:40960\n"
    "v_mfma_f32_16x16x32_bf16 v[52:55], v[172:175], v[204:207], v[52:55]\n"
    "ds_read_b64_tr_b16 v[154:155], v213 offset:41472\n"
    "v_mfma_f32_16x16x32_bf16 v[48:51], v[172:175], v[196:199], v[48:51]\n"
    "ds_read_b64_tr_b16 v[156:157], v213 offset:45056\n"
    "v_mfma_f32_16x16x32_bf16 v[48:51], v[168:171], v[192:195], v[48:51]\n"
    "ds_read_b64_tr_b16 v[158:159], v213 offset:45568\n"
    "v_mfma_f32_16x16x32_bf16 v[96:99], v[176:179], v[192:195], v[96:99]\n"
    "v_mfma_f32_16x16x32_bf16 v[96:99], v[180:183], v[196:199], v[96:99]\n"
    "v_mfma_f32_16x16x32_bf16 v[100:103], v[180:183], v[204:207], v[100:103]\n"
    "v_mfma_f32_16x16x32_bf16 v[100:103], v[176:179], v[200:203], v[100:103]\n"
    "v_mfma_f32_16x16x32_bf16 v[108:111], v[184:187], v[200:203], v[108:111]\n"
    "v_mfma_f32_16x16x32_bf16 v[108:111], v[188:191], v[204:207], v[108:111]\n"
    "v_mfma_f32_16x16x32_bf16 v[104:107], v[188:191], v[196:199], v[104:107]\n"
    "v_mfma_f32_16x16x32_bf16 v[104:107], v[184:187], v[192:195], v[104:107]\n"
    "s_waitcnt lgkmcnt(0)\n"
    "v_mfma_f32_16x16x32_bf16 v[12:15], v[112:115], v[144:147], v[12:15]\n"
    "ds_read_b128 v[200:203], v208 offset:34816\n"
    "v_mfma_f32_16x16x32_bf16 v[12:15], v[116:119], v[148:151], v[12:15]\n"
    "ds_read_b128 v[204:207], v210 offset:34816\n"
    "v_mfma_f32_16x16x32_bf16 v[8:11], v[116:119], v[140:143], v[8:11]\n"
    "ds_read_b128 v[192:195], v208 offset:32768\n"
    "v_mfma_f32_16x16x32_bf16 v[8:11], v[112:115], v[136:139], v[8:11]\n"
    "ds_read_b128 v[196:199], v210 offset:32768\n"
    "v_mfma_f32_16x16x32_bf16 v[24:27], v[120:123], v[136:139], v[24:27]\n"
    "ds_read_b64_tr_b16 v[168:169], v215 offset:16384\n"
    "v_mfma_f32_16x16x32_bf16 v[24:27], v[124:127], v[140:143], v[24:27]\n"
    "ds_read_b64_tr_b16 v[170:171], v215 offset:16896\n"
    "v_mfma_f32_16x16x32_bf16 v[28:31], v[124:127], v[148:151], v[28:31]\n"
    "ds_read_b64_tr_b16 v[172:173], v215 offset:20480\n"
    "v_mfma_f32_16x16x32_bf16 v[28:31], v[120:123], v[144:147], v[28:31]\n"
    "ds_read_b64_tr_b16 v[174:175], v215 offset:20992\n"
    "v_mfma_f32_16x16x32_bf16 v[44:47], v[128:131], v[144:147], v[44:47]\n"
    "ds_read_b64_tr_b16 v[160:161], v212 offset:40960\n"
    "v_mfma_f32_16x16x32_bf16 v[44:47], v[132:135], v[148:151], v[44:47]\n"
    "ds_read_b64_tr_b16 v[162:163], v212 offset:41472\n"
    "v_mfma_f32_16x16x32_bf16 v[40:43], v[132:135], v[140:143], v[40:43]\n"
    "ds_read_b64_tr_b16 v[164:165], v212 offset:45056\n"
    "v_mfma_f32_16x16x32_bf16 v[40:43], v[128:131], v[136:139], v[40:43]\n"
    "ds_read_b64_tr_b16 v[166:167], v212 offset:45568\n"
    "s_waitcnt lgkmcnt(5)\n"
    "v_mfma_f32_16x16x32_bf16 v[36:39], v[128:131], v[200:203], v[36:39]\n"
    "ds_read_b64_tr_b16 v[176:177], v216 offset:8192\n"
    "v_mfma_f32_16x16x32_bf16 v[36:39], v[132:135], v[204:207], v[36:39]\n"
    "ds_read_b64_tr_b16 v[178:179], v216 offset:8448\n"
    "v_mfma_f32_16x16x32_bf16 v[32:35], v[132:135], v[196:199], v[32:35]\n"
    "ds_read_b64_tr_b16 v[180:181], v216 offset:10240\n"
    "v_mfma_f32_16x16x32_bf16 v[32:35], v[128:131], v[192:195], v[32:35]\n"
    "ds_read_b64_tr_b16 v[182:183], v216 offset:10496\n"
    "v_mfma_f32_16x16x32_bf16 v[16:19], v[120:123], v[192:195], v[16:19]\n"
    "ds_read_b64_tr_b16 v[184:185], v217 offset:8192\n"
    "v_mfma_f32_16x16x32_bf16 v[16:19], v[124:127], v[196:199], v[16:19]\n"
    "ds_read_b64_tr_b16 v[186:187], v217 offset:8448\n"
    "v_mfma_f32_16x16x32_bf16 v[20:23], v[124:127], v[204:207], v[20:23]\n"
    "ds_read_b64_tr_b16 v[188:189], v217 offset:10240\n"
    "v_mfma_f32_16x16x32_bf16 v[20:23], v[120:123], v[200:203], v[20:23]\n"
    "ds_read_b64_tr_b16 v[190:191], v217 offset:10496\n"
    "v_mfma_f32_16x16x32_bf16 v[4:7], v[112:115], v[200:203], v[4:7]\n"
    "v_mfma_f32_16x16x32_bf16 v[4:7], v[116:119], v[204:207], v[4:7]\n"
    "v_mfma_f32_16x16x32_bf16 v[0:3], v[116:119], v[196:199], v[0:3]\n"
    "v_mfma_f32_16x16x32_bf16 v[0:3], v[112:115], v[192:195], v[0:3]\n"
    "v_mfma_f32_16x16x32_bf16 v[56:59], v[168:171], v[136:139], v[56:59]\n"
    "s_waitcnt lgkmcnt(0)\n"
    "v_mfma_f32_16x16x32_bf16 v[56:59], v[172:175], v[140:143], v[56:59]\n"
    "v_mfma_f32_16x16x32_bf16 v[60:63], v[172:175], v[148:151], v[60:63]\n"
    "v_mfma_f32_16x16x32_bf16 v[60:63], v[168:171], v[144:147], v[60:63]\n"
    "s_waitcnt vmcnt(0) lgkmcnt(0)\n"
    "s_barrier\n"
    "v_mfma_f32_16x16x32_bf16 v[76:79], v[160:163], v[144:147], v[76:79]\n"
    "ds_read_b64_tr_b16 v[112:113], v212 offset:0\n"
    "v_mfma_f32_16x16x32_bf16 v[76:79], v[164:167], v[148:151], v[76:79]\n"
    "ds_read_b64_tr_b16 v[114:115], v212 offset:512\n"
    "v_mfma_f32_16x16x32_bf16 v[72:75], v[164:167], v[140:143], v[72:75]\n"
    "ds_read_b64_tr_b16 v[116:117], v212 offset:4096\n"
    "v_mfma_f32_16x16x32_bf16 v[72:75], v[160:163], v[136:139], v[72:75]\n"
    "ds_read_b64_tr_b16 v[118:119], v212 offset:4608\n"
    "v_mfma_f32_16x16x32_bf16 v[88:91], v[152:155], v[136:139], v[88:91]\n"
    "ds_read_b64_tr_b16 v[120:121], v213 offset:0\n"
    "v_mfma_f32_16x16x32_bf16 v[88:91], v[156:159], v[140:143], v[88:91]\n"
    "ds_read_b64_tr_b16 v[122:123], v213 offset:512\n"
    "v_mfma_f32_16x16x32_bf16 v[92:95], v[156:159], v[148:151], v[92:95]\n"
    "ds_read_b64_tr_b16 v[124:125], v213 offset:4096\n"
    "v_mfma_f32_16x16x32_bf16 v[92:95], v[152:155], v[144:147], v[92:95]\n"
    "ds_read_b64_tr_b16 v[126:127], v213 offset:4608\n"
    "v_mfma_f32_16x16x32_bf16 v[84:87], v[152:155], v[200:203], v[84:87]\n"
    "ds_read_b128 v[144:147], v209 offset:2048\n"
    "v_mfma_f32_16x16x32_bf16 v[84:87], v[156:159], v[204:207], v[84:87]\n"
    "ds_read_b128 v[148:151], v211 offset:2048\n"
    "v_mfma_f32_16x16x32_bf16 v[80:83], v[156:159], v[196:199], v[80:83]\n"
    "ds_read_b128 v[136:139], v209 offset:0\n"
    "v_mfma_f32_16x16x32_bf16 v[80:83], v[152:155], v[192:195], v[80:83]\n"
    "ds_read_b128 v[140:143], v211 offset:0\n"
    "v_mfma_f32_16x16x32_bf16 v[64:67], v[160:163], v[192:195], v[64:67]\n"
    "ds_read_b64_tr_b16 v[128:129], v214 offset:0\n"
    "v_mfma_f32_16x16x32_bf16 v[64:67], v[164:167], v[196:199], v[64:67]\n"
    "ds_read_b64_tr_b16 v[130:131], v214 offset:512\n"
    "v_mfma_f32_16x16x32_bf16 v[68:71], v[164:167], v[204:207], v[68:71]\n"
    "ds_read_b64_tr_b16 v[132:133], v214 offset:4096\n"
    "v_mfma_f32_16x16x32_bf16 v[68:71], v[160:163], v[200:203], v[68:71]\n"
    "ds_read_b64_tr_b16 v[134:135], v214 offset:4608\n"
    "v_mfma_f32_16x16x32_bf16 v[52:55], v[168:171], v[200:203], v[52:55]\n"
    "ds_read_b64_tr_b16 v[152:153], v213 offset:24576\n"
    "v_mfma_f32_16x16x32_bf16 v[52:55], v[172:175], v[204:207], v[52:55]\n"
    "ds_read_b64_tr_b16 v[154:155], v213 offset:25088\n"
    "v_mfma_f32_16x16x32_bf16 v[48:51], v[172:175], v[196:199], v[48:51]\n"
    "ds_read_b64_tr_b16 v[156:157], v213 offset:28672\n"
    "v_mfma_f32_16x16x32_bf16 v[48:51], v[168:171], v[192:195], v[48:51]\n"
    "ds_read_b64_tr_b16 v[158:159], v213 offset:29184\n"
    "v_mfma_f32_16x16x32_bf16 v[96:99], v[176:179], v[192:195], v[96:99]\n"
    "v_mfma_f32_16x16x32_bf16 v[96:99], v[180:183], v[196:199], v[96:99]\n"
    "v_mfma_f32_16x16x32_bf16 v[100:103], v[180:183], v[204:207], v[100:103]\n"
    "v_mfma_f32_16x16x32_bf16 v[100:103], v[176:179], v[200:203], v[100:103]\n"
    "v_mfma_f32_16x16x32_bf16 v[108:111], v[184:187], v[200:203], v[108:111]\n"
    "v_mfma_f32_16x16x32_bf16 v[108:111], v[188:191], v[204:207], v[108:111]\n"
    "v_mfma_f32_16x16x32_bf16 v[104:107], v[188:191], v[196:199], v[104:107]\n"
    "v_mfma_f32_16x16x32_bf16 v[104:107], v[184:187], v[192:195], v[104:107]\n"
    "s_waitcnt lgkmcnt(0)\n"
    "v_mfma_f32_16x16x32_bf16 v[12:15], v[112:115], v[144:147], v[12:15]\n"
    "ds_read_b128 v[200:203], v208 offset:2048\n"
    "v_mfma_f32_16x16x32_bf16 v[12:15], v[116:119], v[148:151], v[12:15]\n"
    "ds_read_b128 v[204:207], v210 offset:2048\n"
    "v_mfma_f32_16x16x32_bf16 v[8:11], v[116:119], v[140:143], v[8:11]\n"
    "ds_read_b128 v[192:195], v208 offset:0\n"
    "v_mfma_f32_16x16x32_bf16 v[8:11], v[112:115], v[136:139], v[8:11]\n"
    "ds_read_b128 v[196:199], v210 offset:0\n"
    "v_mfma_f32_16x16x32_bf16 v[24:27], v[120:123], v[136:139], v[24:27]\n"
    "ds_read_b64_tr_b16 v[168:169], v215 offset:0\n"
    "v_mfma_f32_16x16x32_bf16 v[24:27], v[124:127], v[140:143], v[24:27]\n"
    "ds_read_b64_tr_b16 v[170:171], v215 offset:512\n"
    "v_mfma_f32_16x16x32_bf16 v[28:31], v[124:127], v[148:151], v[28:31]\n"
    "ds_read_b64_tr_b16 v[172:173], v215 offset:4096\n"
    "v_mfma_f32_16x16x32_bf16 v[28:31], v[120:123], v[144:147], v[28:31]\n"
    "ds_read_b64_tr_b16 v[174:175], v215 offset:4608\n"
    "v_mfma_f32_16x16x32_bf16 v[44:47], v[128:131], v[144:147], v[44:47]\n"
    "ds_read_b64_tr_b16 v[160:161], v212 offset:24576\n"
    "v_mfma_f32_16x16x32_bf16 v[44:47], v[132:135], v[148:151], v[44:47]\n"
    "ds_read_b64_tr_b16 v[162:163], v212 offset:25088\n"
    "v_mfma_f32_16x16x32_bf16 v[40:43], v[132:135], v[140:143], v[40:43]\n"
    "ds_read_b64_tr_b16 v[164:165], v212 offset:28672\n"
    "v_mfma_f32_16x16x32_bf16 v[40:43], v[128:131], v[136:139], v[40:43]\n"
    "ds_read_b64_tr_b16 v[166:167], v212 offset:29184\n"
    "s_waitcnt lgkmcnt(5)\n"
    "v_mfma_f32_16x16x32_bf16 v[36:39], v[128:131], v[200:203], v[36:39]\n"
    "ds_read_b64_tr_b16 v[176:177], v216 offset:0\n"
    "v_mfma_f32_16x16x32_bf16 v[36:39], v[132:135], v[204:207], v[36:39]\n"
    "ds_read_b64_tr_b16 v[178:179], v216 offset:256\n"
    "v_mfma_f32_16x16x32_bf16 v[32:35], v[132:135], v[196:199], v[32:35]\n"
    "ds_read_b64_tr_b16 v[180:181], v216 offset:2048\n"
    "v_mfma_f32_16x16x32_bf16 v[32:35], v[128:131], v[192:195], v[32:35]\n"
    "ds_read_b64_tr_b16 v[182:183], v216 offset:2304\n"
    "v_mfma_f32_16x16x32_bf16 v[16:19], v[120:123], v[192:195], v[16:19]\n"
    "ds_read_b64_tr_b16 v[184:185], v217 offset:0\n"
    "v_mfma_f32_16x16x32_bf16 v[16:19], v[124:127], v[196:199], v[16:19]\n"
    "ds_read_b64_tr_b16 v[186:187], v217 offset:256\n"
    "v_mfma_f32_16x16x32_bf16 v[20:23], v[124:127], v[204:207], v[20:23]\n"
    "ds_read_b64_tr_b16 v[188:189], v217 offset:2048\n"
    "v_mfma_f32_16x16x32_bf16 v[20:23], v[120:123], v[200:203], v[20:23]\n"
    "ds_read_b64_tr_b16 v[190:191], v217 offset:2304\n"
    "v_mfma_f32_16x16x32_bf16 v[4:7], v[112:115], v[200:203], v[4:7]\n"
    "v_mfma_f32_16x16x32_bf16 v[4:7], v[116:119], v[204:207], v[4:7]\n"
    "v_mfma_f32_16x16x32_bf16 v[0:3], v[116:119], v[196:199], v[0:3]\n"
    "v_mfma_f32_16x16x32_bf16 v[0:3], v[112:115], v[192:195], v[0:3]\n"
    "v_mfma_f32_16x16x32_bf16 v[56:59], v[168:171], v[136:139], v[56:59]\n"
    "s_waitcnt lgkmcnt(0)\n"
    "v_mfma_f32_16x16x32_bf16 v[56:59], v[172:175], v[140:143], v[56:59]\n"
    "s_sub_u32 s58, s58, 1\n"
    "v_mfma_f32_16x16x32_bf16 v[60:63], v[172:175], v[148:151], v[60:63]\n"
    "s_cmp_lg_u32 s58, 0\n"
    "v_mfma_f32_16x16x32_bf16 v[60:63], v[168:171], v[144:147], v[60:63]\n"
    "s_waitcnt lgkmcnt(0)\n"
    "s_cbranch_scc1 Lg1_%=\n"
    "s_barrier\n"
    "v_mfma_f32_16x16x32_bf16 v[76:79], v[160:163], v[144:147], v[76:79]\n"
    "ds_read_b128 v[112:115], v224 offset:0\n"
    "ds_read_b128 v[116:119], v224 offset:256\n"
    "v_mfma_f32_16x16x32_bf16 v[76:79], v[164:167], v[148:151], v[76:79]\n"
    "ds_read_b128 v[120:123], v230 offset:0\n"
    "ds_read_b128 v[124:127], v230 offset:256\n"
    "v_mfma_f32_16x16x32_bf16 v[72:75], v[164:167], v[140:143], v[72:75]\n"
    "ds_read_b128 v[128:131], v225 offset:0\n"
    "ds_read_b128 v[132:135], v225 offset:16\n"
    "v_mfma_f32_16x16x32_bf16 v[72:75], v[160:163], v[136:139], v[72:75]\n"
    "v_mfma_f32_16x16x32_bf16 v[88:91], v[152:155], v[136:139], v[88:91]\n"
    "v_mfma_f32_16x16x32_bf16 v[88:91], v[156:159], v[140:143], v[88:91]\n"
    "v_mfma_f32_16x16x32_bf16 v[92:95], v[156:159], v[148:151], v[92:95]\n"
    "s_waitcnt lgkmcnt(1)\n"
    "v_fmac_f32 v0, v112, v128\n"
    "v_fmac_f32 v1, v112, v129\n"
    "v_mfma_f32_16x16x32_bf16 v[92:95], v[152:155], v[144:147], v[92:95]\n"
    "v_fmac_f32 v2, v112, v130\n"
    "v_fmac_f32 v3, v112, v131\n"
    "v_mfma_f32_16x16x32_bf16 v[84:87], v[152:155], v[200:203], v[84:87]\n"
    "ds_read_b128 v[136:139], v225 offset:32\n"
    "ds_read_b128 v[140:143], v225 offset:256\n"
    "v_mfma_f32_16x16x32_bf16 v[84:87], v[156:159], v[204:207], v[84:87]\n"
    "ds_read_b128 v[144:147], v225 offset:272\n"
    "ds_read_b128 v[148:151], v225 offset:288\n"
    "v_mfma_f32_16x16x32_bf16 v[80:83], v[156:159], v[196:199], v[80:83]\n"
    "s_waitcnt lgkmcnt(4)\n"
    "v_fmac_f32 v16, v112, v132\n"
    "v_fmac_f32 v17, v112, v133\n"
    "v_mfma_f32_16x16x32_bf16 v[80:83], v[152:155], v[192:195], v[80:83]\n"
    "v_fmac_f32 v18, v112, v134\n"
    "v_fmac_f32 v19, v112, v135\n"
    "v_mfma_f32_16x16x32_bf16 v[64:67], v[160:163], v[192:195], v[64:67]\n"
    "ds_read_b128 v[152:155], v226 offset:0\n"
    "ds_read_b128 v[156:159], v226 offset:256\n"
    "v_mfma_f32_16x16x32_bf16 v[64:67], v[164:167], v[196:199], v[64:67]\n"
    "v_fmac_f32 v4, v116, v128\n"
    "v_fmac_f32 v5, v116, v129\n"
    "v_mfma_f32_16x16x32_bf16 v[68:71], v[164:167], v[204:207], v[68:71]\n"
    "v_fmac_f32 v6, v116, v130\n"
    "v_fmac_f32 v7, v116, v131\n"
    "v_mfma_f32_16x16x32_bf16 v[68:71], v[160:163], v[200:203], v[68:71]\n"
    "v_fmac_f32 v20, v116, v132\n"
    "v_fmac_f32 v21, v116, v133\n"
    "v_mfma_f32_16x16x32_bf16 v[52:55], v[168:171], v[200:203], v[52:55]\n"
    "v_fmac_f32 v22, v116, v134\n"
    "v_fmac_f32 v23, v116, v135\n"
    "v_mfma_f32_16x16x32_bf16 v[52:55], v[172:175], v[204:207], v[52:55]\n"
    "v_fmac_f32 v8, v120, v128\n"
    "v_fmac_f32 v9, v120, v129\n"
    "v_mfma_f32_16x16x32_bf16 v[48:51], v[172:175], v[196:199], v[48:51]\n"
    "v_fmac_f32 v10, v120, v130\n"
    "v_fmac_f32 v11, v120, v131\n"
    "v_mfma_f32_16x16x32_bf16 v[48:51], v[168:171], v[192:195], v[48:51]\n"
    "v_fmac_f32 v24, v120, v132\n"
    "v_fmac_f32 v25, v120, v133\n"
    "v_mfma_f32_16x16x32_bf16 v[96:99], v[176:179], v[192:195], v[96:99]\n"
    "v_fmac_f32 v26, v120, v134\n"
    "v_fmac_f32 v27, v120, v135\n"
    "v_mfma_f32_16x16x32_bf16 v[96:99], v[180:183], v[196:199], v[96:99]\n"
    "v_fmac_f32 v12, v124, v128\n"
    "v_fmac_f32 v13, v124, v129\n"
    "v_mfma_f32_16x16x32_bf16 v[100:103], v[180:183], v[204:207], v[100:103]\n"
    "v_fmac_f32 v14, v124, v130\n"
    "v_fmac_f32 v15, v124, v131\n"
    "v_mfma_f32_16x16x32_bf16 v[100:103], v[176:179], v[200:203], v[100:103]\n"
    "v_fmac_f32 v28, v124, v132\n"
    "v_fmac_f32 v29, v124, v133\n"
    "v_mfma_f32_16x16x32_bf16 v[108:111], v[184:187], v[200:203], v[108:111]\n"
    "v_fmac_f32 v30, v124, v134\n"
    "v_fmac_f32 v31, v124, v135\n"
    "v_mfma_f32_16x16x32_bf16 v[108:111], v[188:191], v[204:207], v[108:111]\n"
    "v_mfma_f32_16x16x32_bf16 v[104:107], v[188:191], v[196:199], v[104:107]\n"
    "v_mfma_f32_16x16x32_bf16 v[104:107], v[184:187], v[192:195], v[104:107]\n"
    "s_waitcnt lgkmcnt(0)\n"
    "v_pk_mul_f32 v[0:1], v[114:115], v[0:1] op_sel:[1,0]\n"
    "v_pk_mul_f32 v[2:3], v[114:115], v[2:3] op_sel:[1,0]\n"
    "v_pk_mul_f32 v[16:17], v[114:115], v[16:17] op_sel:[1,0]\n"
    "v_pk_mul_f32 v[18:19], v[114:115], v[18:19] op_sel:[1,0]\n"
    "v_exp_f32 v160, v0\n"
    "v_exp_f32 v161, v1\n"
    "v_exp_f32 v162, v2\n"
    "v_exp_f32 v163, v3\n"
    "v_exp_f32 v164, v16\n"
    "v_exp_f32 v165, v17\n"
    "v_exp_f32 v166, v18\n"
    "v_exp_f32 v167, v19\n"
    "v_pk_add_f32 v[160:161], v[160:161], 1.0 op_sel_hi:[1,0]\n"
    "v_pk_add_f32 v[162:163], v[162:163], 1.0 op_sel_hi:[1,0]\n"
    "v_pk_add_f32 v[164:165], v[164:165], 1.0 op_sel_hi:[1,0]\n"
    "v_pk_add_f32 v[166:167], v[166:167], 1.0 op_sel_hi:[1,0]\n"
    "v_rcp_f32 v160, v160\n"
    "v_rcp_f32 v161, v161\n"
    "v_rcp_f32 v162, v162\n"
    "v_rcp_f32 v163, v163\n"
    "v_rcp_f32 v164, v164\n"
    "v_rcp_f32 v165, v165\n"
    "v_rcp_f32 v166, v166\n"
    "v_rcp_f32 v167, v167\n"
    "v_pk_mul_f32 v[160:161], v[0:1], v[160:161]\n"
    "v_pk_mul_f32 v[162:163], v[2:3], v[162:163]\n"
    "v_pk_mul_f32 v[164:165], v[16:17], v[164:165]\n"
    "v_pk_mul_f32 v[166:167], v[18:19], v[166:167]\n"
    "v_cvt_pk_bf16_f32 v176, v160, v161\n"
    "v_cvt_pk_bf16_f32 v177, v162, v163\n"
    "v_cvt_pk_bf16_f32 v178, v164, v165\n"
    "v_cvt_pk_bf16_f32 v179, v166, v167\n"
    "buffer_store_dwordx4 v[176:179], v238, s[24:27], s60 offen offset:0 sc1\n"
    "ds_write_b128 v238, v[176:179] offset:0\n"
    "v_pk_mul_f32 v[4:5], v[118:119], v[4:5] op_sel:[1,0]\n"
    "v_pk_mul_f32 v[6:7], v[118:119], v[6:7] op_sel:[1,0]\n"
    "v_pk_mul_f32 v[20:21], v[118:119], v[20:21] op_sel:[1,0]\n"
    "v_pk_mul_f32 v[22:23], v[118:119], v[22:23] op_sel:[1,0]\n"
    "v_exp_f32 v168, v4\n"
    "v_exp_f32 v169, v5\n"
    "v_exp_f32 v170, v6\n"
    "v_exp_f32 v171, v7\n"
    "v_exp_f32 v172, v20\n"
    "v_exp_f32 v173, v21\n"
    "v_exp_f32 v174, v22\n"
    "v_exp_f32 v175, v23\n"
    "v_pk_add_f32 v[168:169], v[168:169], 1.0 op_sel_hi:[1,0]\n"
    "v_pk_add_f32 v[170:171], v[170:171], 1.0 op_sel_hi:[1,0]\n"
    "v_pk_add_f32 v[172:173], v[172:173], 1.0 op_sel_hi:[1,0]\n"
    "v_pk_add_f32 v[174:175], v[174:175], 1.0 op_sel_hi:[1,0]\n"
    "v_rcp_f32 v168, v168\n"
    "v_rcp_f32 v169, v169\n"
    "v_rcp_f32 v170, v170\n"
    "v_rcp_f32 v171, v171\n"
    "v_rcp_f32 v172, v172\n"
    "v_rcp_f32 v173, v173\n"
    "v_rcp_f32 v174, v174\n"
    "v_rcp_f32 v175, v175\n"
    "v_pk_mul_f32 v[168:169], v[4:5], v[168:169]\n"
    "v_pk_mul_f32 v[170:171], v[6:7], v[170:171]\n"
    "v_pk_mul_f32 v[172:173], v[20:21], v[172:173]\n"
    "v_pk_mul_f32 v[174:175], v[22:23], v[174:175]\n"
    "v_cvt_pk_bf16_f32 v180, v168, v169\n"
    "v_cvt_pk_bf16_f32 v181, v170, v171\n"
    "v_cvt_pk_bf16_f32 v182, v172, v173\n"
    "v_cvt_pk_bf16_f32 v183, v174, v175\n"
    "buffer_store_dwordx4 v[180:183], v238, s[24:27], s60 offen offset:1024 sc1\n"
    "ds_write_b128 v238, v[180:183] offset:1024\n"
    "v_pk_mul_f32 v[8:9], v[122:123], v[8:9] op_sel:[1,0]\n"
    "v_pk_mul_f32 v[10:11], v[122:123], v[10:11] op_sel:[1,0]\n"
    "v_pk_mul_f32 v[24:25], v[122:123], v[24:25] op_sel:[1,0]\n"
    "v_pk_mul_f32 v[26:27], v[122:123], v[26:27] op_sel:[1,0]\n"
    "v_exp_f32 v160, v8\n"
    "v_exp_f32 v161, v9\n"
    "v_exp_f32 v162, v10\n"
    "v_exp_f32 v163, v11\n"
    "v_exp_f32 v164, v24\n"
    "v_exp_f32 v165, v25\n"
    "v_exp_f32 v166, v26\n"
    "v_exp_f32 v167, v27\n"
    "v_pk_add_f32 v[160:161], v[160:161], 1.0 op_sel_hi:[1,0]\n"
    "v_pk_add_f32 v[162:163], v[162:163], 1.0 op_sel_hi:[1,0]\n"
    "v_pk_add_f32 v[164:165], v[164:165], 1.0 op_sel_hi:[1,0]\n"
    "v_pk_add_f32 v[166:167], v[166:167], 1.0 op_sel_hi:[1,0]\n"
    "v_rcp_f32 v160, v160\n"
    "v_rcp_f32 v161, v161\n"
    "v_rcp_f32 v162, v162\n"
    "v_rcp_f32 v163, v163\n"
    "v_rcp_f32 v164, v164\n"
    "v_rcp_f32 v165, v165\n"
    "v_rcp_f32 v166, v166\n"
    "v_rcp_f32 v167, v167\n"
    "v_pk_mul_f32 v[160:161], v[8:9], v[160:161]\n"
    "v_pk_mul_f32 v[162:163], v[10:11], v[162:163]\n"
    "v_pk_mul_f32 v[164:165], v[24:25], v[164:165]\n"
    "v_pk_mul_f32 v[166:167], v[26:27], v[166:167]\n"
    "v_cvt_pk_bf16_f32 v184, v160, v161\n"
    "v_cvt_pk_bf16_f32 v185, v162, v163\n"
    "v_cvt_pk_bf16_f32 v186, v164, v165\n"
    "v_cvt_pk_bf16_f32 v187, v166, v167\n"
    "buffer_store_dwordx4 v[184:187], v239, s[24:27], s60 offen offset:0 sc1\n"
    "ds_write_b128 v239, v[184:187] offset:0\n"
    "v_mul_f32 v12, v127, v12\n"
    "v_mul_f32 v13, v127, v13\n"
    "v_mul_f32 v14, v127, v14\n"
    "v_mul_f32 v15, v127, v15\n"
    "v_mul_f32 v28, v127, v28\n"
    "v_mul_f32 v29, v127, v29\n"
    "v_mul_f32 v30, v127, v30\n"
    "v_mul_f32 v31, v127, v31\n"
    "v_exp_f32 v168, v12\n"
    "v_exp_f32 v169, v13\n"
    "v_exp_f32 v170, v14\n"
    "v_exp_f32 v171, v15\n"
    "v_exp_f32 v172, v28\n"
    "v_exp_f32 v173, v29\n"
    "v_exp_f32 v174, v30\n"
    "v_exp_f32 v175, v31\n"
    "v_add_f32 v168, 1.0, v168\n"
    "v_add_f32 v169, 1.0, v169\n"
    "v_add_f32 v170, 1.0, v170\n"
    "v_add_f32 v171, 1.0, v171\n"
    "v_add_f32 v172, 1.0, v172\n"
    "v_add_f32 v173, 1.0, v173\n"
    "v_add_f32 v174, 1.0, v174\n"
    "v_add_f32 v175, 1.0, v175\n"
    "v_rcp_f32 v168, v168\n"
    "v_rcp_f32 v169, v169\n"
    "v_rcp_f32 v170, v170\n"
    "v_rcp_f32 v171, v171\n"
    "v_rcp_f32 v172, v172\n"
    "v_rcp_f32 v173, v173\n"
    "v_rcp_f32 v174, v174\n"
    "v_rcp_f32 v175, v175\n"
    "v_mul_f32 v168, v12, v168\n"
    "v_mul_f32 v169, v13, v169\n"
    "v_mul_f32 v170, v14, v170\n"
    "v_mul_f32 v171, v15, v171\n"
    "v_mul_f32 v172, v28, v172\n"
    "v_mul_f32 v173, v29, v173\n"
    "v_mul_f32 v174, v30, v174\n"
    "v_mul_f32 v175, v31, v175\n"
    "v_cvt_pk_bf16_f32 v176, v168, v169\n"
    "v_cvt_pk_bf16_f32 v177, v170, v171\n"
    "v_cvt_pk_bf16_f32 v178, v172, v173\n"
    "v_cvt_pk_bf16_f32 v179, v174, v175\n"
    "buffer_store_dwordx4 v[176:179], v239, s[24:27], s60 offen offset:1024 sc1\n"
    "ds_write_b128 v239, v[176:179] offset:1024\n"
    "v_fmac_f32 v32, v112, v136\n"
    "v_fmac_f32 v33, v112, v137\n"
    "v_fmac_f32 v34, v112, v138\n"
    "v_fmac_f32 v35, v112, v139\n"
    "v_fmac_f32 v48, v112, v140\n"
    "v_fmac_f32 v49, v112, v141\n"
    "v_fmac_f32 v50, v112, v142\n"
    "v_fmac_f32 v51, v112, v143\n"
    "v_mul_f32 v32, v115, v32\n"
    "v_mul_f32 v33, v115, v33\n"
    "v_mul_f32 v34, v115, v34\n"
    "v_mul_f32 v35, v115, v35\n"
    "v_mul_f32 v48, v115, v48\n"
    "v_mul_f32 v49, v115, v49\n"
    "v_mul_f32 v50, v115, v50\n"
    "v_mul_f32 v51, v115, v51\n"
    "v_exp_f32 v160, v32\n"
    "v_exp_f32 v161, v33\n"
    "v_exp_f32 v162, v34\n"
    "v_exp_f32 v163, v35\n"
    "v_exp_f32 v164, v48\n"
    "v_exp_f32 v165, v49\n"
    "v_exp_f32 v166, v50\n"
    "v_exp_f32 v167, v51\n"
    "v_add_f32 v160, 1.0, v160\n"
    "v_add_f32 v161, 1.0, v161\n"
    "v_add_f32 v162, 1.0, v162\n"
    "v_add_f32 v163, 1.0, v163\n"
    "v_add_f32 v164, 1.0, v164\n"
    "v_add_f32 v165, 1.0, v165\n"
    "v_add_f32 v166, 1.0, v166\n"
    "v_add_f32 v167, 1.0, v167\n"
    "v_rcp_f32 v160, v160\n"
    "v_rcp_f32 v161, v161\n"
    "v_rcp_f32 v162, v162\n"
    "v_rcp_f32 v163, v163\n"
    "v_rcp_f32 v164, v164\n"
    "v_rcp_f32 v165, v165\n"
    "v_rcp_f32 v166, v166\n"
    "v_rcp_f32 v167, v167\n"
    "v_mul_f32 v160, v32, v160\n"
    "v_mul_f32 v161, v33, v161\n"
    "v_mul_f32 v162, v34, v162\n"
    "v_mul_f32 v163, v35, v163\n"
    "v_mul_f32 v164, v48, v164\n"
    "v_mul_f32 v165, v49, v165\n"
    "v_mul_f32 v166, v50, v166\n"
    "v_mul_f32 v167, v51, v167\n"
    "v_cvt_pk_bf16_f32 v180, v160, v161\n"
    "v_cvt_pk_bf16_f32 v181, v162, v163\n"
    "v_cvt_pk_bf16_f32 v182, v164, v165\n"
    "v_cvt_pk_bf16_f32 v183, v166, v167\n"
    "s_add_u32 s51, s60, 8192\n"
    "buffer_store_dwordx4 v[180:183], v238, s[24:27], s51 offen offset:0 sc1\n"
    "ds_write_b128 v238, v[180:183] offset:8192\n"
    "v_fmac_f32 v36, v116, v136\n"
    "v_fmac_f32 v37, v116, v137\n"
    "v_fmac_f32 v38, v116, v138\n"
    "v_fmac_f32 v39, v116, v139\n"
    "v_fmac_f32 v52, v116, v140\n"
    "v_fmac_f32 v53, v116, v141\n"
    "v_fmac_f32 v54, v116, v142\n"
    "v_fmac_f32 v55, v116, v143\n"
    "v_mul_f32 v36, v119, v36\n"
    "v_mul_f32 v37, v119, v37\n"
    "v_mul_f32 v38, v119, v38\n"
    "v_mul_f32 v39, v119, v39\n"
    "v_mul_f32 v52, v119, v52\n"
    "v_mul_f32 v53, v119, v53\n"
    "v_mul_f32 v54, v119, v54\n"
    "v_mul_f32 v55, v119, v55\n"
    "v_exp_f32 v168, v36\n"
    "v_exp_f32 v169, v37\n"
    "v_exp_f32 v170, v38\n"
    "v_exp_f32 v171, v39\n"
    "v_exp_f32 v172, v52\n"
    "v_exp_f32 v173, v53\n"
    "v_exp_f32 v174, v54\n"
    "v_exp_f32 v175, v55\n"
    "v_add_f32 v168, 1.0, v168\n"
    "v_add_f32 v169, 1.0, v169\n"
    "v_add_f32 v170, 1.0, v170\n"
    "v_add_f32 v171, 1.0, v171\n"
    "v_add_f32 v172, 1.0, v172\n"
    "v_add_f32 v173, 1.0, v173\n"
    "v_add_f32 v174, 1.0, v174\n"
    "v_add_f32 v175, 1.0, v175\n"
    "v_rcp_f32 v168, v168\n"
    "v_rcp_f32 v169, v169\n"
    "v_rcp_f32 v170, v170\n"
    "v_rcp_f32 v171, v171\n"
    "v_rcp_f32 v172, v172\n"
    "v_rcp_f32 v173, v173\n"
    "v_rcp_f32 v174, v174\n"
    "v_rcp_f32 v175, v175\n"
    "v_mul_f32 v168, v36, v168\n"
    "v_mul_f32 v169, v37, v169\n"
    "v_mul_f32 v170, v38, v170\n"
    "v_mul_f32 v171, v39, v171\n"
    "v_mul_f32 v172, v52, v172\n"
    "v_mul_f32 v173, v53, v173\n"
    "v_mul_f32 v174, v54, v174\n"
    "v_mul_f32 v175, v55, v175\n"
    "v_cvt_pk_bf16_f32 v184, v168, v169\n"
    "v_cvt_pk_bf16_f32 v185, v170, v171\n"
    "v_cvt_pk_bf16_f32 v186, v172, v173\n"
    "v_cvt_pk_bf16_f32 v187, v174, v175\n"
    "buffer_store_dwordx4 v[184:187], v238, s[24:27], s51 offen offset:1024 sc1\n"
    "ds_write_b128 v238, v[184:187] offset:9216\n"
    "v_fmac_f32 v40, v120, v136\n"
    "v_fmac_f32 v41, v120, v137\n"
    "v_fmac_f32 v42, v120, v138\n"
    "v_fmac_f32 v43, v120, v139\n"
    "v_fmac_f32 v56, v120, v140\n"
    "v_fmac_f32 v57, v120, v141\n"
    "v_fmac_f32 v58, v120, v142\n"
    "v_fmac_f32 v59, v120, v143\n"
    "v_mul_f32 v40, v123, v40\n"
    "v_mul_f32 v41, v123, v41\n"
    "v_mul_f32 v42, v123, v42\n"
    "v_mul_f32 v43, v123, v43\n"
    "v_mul_f32 v56, v123, v56\n"
    "v_mul_f32 v57, v123, v57\n"
    "v_mul_f32 v58, v123, v58\n"
    "v_mul_f32 v59, v123, v59\n"
    "v_exp_f32 v160, v40\n"
    "v_exp_f32 v161, v41\n"
    "v_exp_f32 v162, v42\n"
    "v_exp_f32 v163, v43\n"
    "v_exp_f32 v164, v56\n"
    "v_exp_f32 v165, v57\n"
    "v_exp_f32 v166, v58\n"
    "v_exp_f32 v167, v59\n"
    "v_add_f32 v160, 1.0, v160\n"
    "v_add_f32 v161, 1.0, v161\n"
    "v_add_f32 v162, 1.0, v162\n"
    "v_add_f32 v163, 1.0, v163\n"
    "v_add_f32 v164, 1.0, v164\n"
    "v_add_f32 v165, 1.0, v165\n"
    "v_add_f32 v166, 1.0, v166\n"
    "v_add_f32 v167, 1.0, v167\n"
    "v_rcp_f32 v160, v160\n"
    "v_rcp_f32 v161, v161\n"
    "v_rcp_f32 v162, v162\n"
    "v_rcp_f32 v163, v163\n"
    "v_rcp_f32 v164, v164\n"
    "v_rcp_f32 v165, v165\n"
    "v_rcp_f32 v166, v166\n"
    "v_rcp_f32 v167, v167\n"
    "v_mul_f32 v160, v40, v160\n"
    "v_mul_f32 v161, v41, v161\n"
    "v_mul_f32 v162, v42, v162\n"
    "v_mul_f32 v163, v43, v163\n"
    "v_mul_f32 v164, v56, v164\n"
    "v_mul_f32 v165, v57, v165\n"
    "v_mul_f32 v166, v58, v166\n"
    "v_mul_f32 v167, v59, v167\n"
    "v_cvt_pk_bf16_f32 v176, v160, v161\n"
    "v_cvt_pk_bf16_f32 v177, v162, v163\n"
    "v_cvt_pk_bf16_f32 v178, v164, v165\n"
    "v_cvt_pk_bf16_f32 v179, v166, v167\n"
    "buffer_store_dwordx4 v[176:179], v239, s[24:27], s51 offen offset:0 sc1\n"
    "ds_write_b128 v239, v[176:179] offset:8192\n"
    "v_fmac_f32 v44, v124, v136\n"
    "v_fmac_f32 v45, v124, v137\n"
    "v_fmac_f32 v46, v124, v138\n"
    "v_fmac_f32 v47, v124, v139\n"
    "v_fmac_f32 v60, v124, v140\n"
    "v_fmac_f32 v61, v124, v141\n"
    "v_fmac_f32 v62, v124, v142\n"
    "v_fmac_f32 v63, v124, v143\n"
    "v_mul_f32 v44, v127, v44\n"
    "v_mul_f32 v45, v127, v45\n"
    "v_mul_f32 v46, v127, v46\n"
    "v_mul_f32 v47, v127, v47\n"
    "v_mul_f32 v60, v127, v60\n"
    "v_mul_f32 v61, v127, v61\n"
    "v_mul_f32 v62, v127, v62\n"
    "v_mul_f32 v63, v127, v63\n"
    "v_exp_f32 v168, v44\n"
    "v_exp_f32 v169, v45\n"
    "v_exp_f32 v170, v46\n"
    "v_exp_f32 v171, v47\n"
    "v_exp_f32 v172, v60\n"
    "v_exp_f32 v173, v61\n"
    "v_exp_f32 v174, v62\n"
    "v_exp_f32 v175, v63\n"
    "v_add_f32 v168, 1.0, v168\n"
    "v_add_f32 v169, 1.0, v169\n"
    "v_add_f32 v170, 1.0, v170\n"
    "v_add_f32 v171, 1.0, v171\n"
    "v_add_f32 v172, 1.0, v172\n"
    "v_add_f32 v173, 1.0, v173\n"
    "v_add_f32 v174, 1.0, v174\n"
    "v_add_f32 v175, 1.0, v175\n"
    "v_rcp_f32 v168, v168\n"
    "v_rcp_f32 v169, v169\n"
    "v_rcp_f32 v170, v170\n"
    "v_rcp_f32 v171, v171\n"
    "v_rcp_f32 v172, v172\n"
    "v_rcp_f32 v173, v173\n"
    "v_rcp_f32 v174, v174\n"
    "v_rcp_f32 v175, v175\n"
    "v_mul_f32 v168, v44, v168\n"
    "v_mul_f32 v169, v45, v169\n"
    "v_mul_f32 v170, v46, v170\n"
    "v_mul_f32 v171, v47, v171\n"
    "v_mul_f32 v172, v60, v172\n"
    "v_mul_f32 v173, v61, v173\n"
    "v_mul_f32 v174, v62, v174\n"
    "v_mul_f32 v175, v63, v175\n"
    "v_cvt_pk_bf16_f32 v180, v168, v169\n"
    "v_cvt_pk_bf16_f32 v181, v170, v171\n"
    "v_cvt_pk_bf16_f32 v182, v172, v173\n"
    "v_cvt_pk_bf16_f32 v183, v174, v175\n"
    "buffer_store_dwordx4 v[180:183], v239, s[24:27], s51 offen offset:1024 sc1\n"
    "ds_write_b128 v239, v[180:183] offset:9216\n"
    "v_fmac_f32 v64, v112, v144\n"
    "v_fmac_f32 v65, v112, v145\n"
    "v_fmac_f32 v66, v112, v146\n"
    "v_fmac_f32 v67, v112, v147\n"
    "v_fmac_f32 v80, v112, v148\n"
    "v_fmac_f32 v81, v112, v149\n"
    "v_fmac_f32 v82, v112, v150\n"
    "v_fmac_f32 v83, v112, v151\n"
    "v_mul_f32 v64, v115, v64\n"
    "v_mul_f32 v65, v115, v65\n"
    "v_mul_f32 v66, v115, v66\n"
    "v_mul_f32 v67, v115, v67\n"
    "v_mul_f32 v80, v115, v80\n"
    "v_mul_f32 v81, v115, v81\n"
    "v_mul_f32 v82, v115, v82\n"
    "v_mul_f32 v83, v115, v83\n"
    "v_exp_f32 v160, v64\n"
    "v_exp_f32 v161, v65\n"
    "v_exp_f32 v162, v66\n"
    "v_exp_f32 v163, v67\n"
    "v_exp_f32 v164, v80\n"
    "v_exp_f32 v165, v81\n"
    "v_exp_f32 v166, v82\n"
    "v_exp_f32 v167, v83\n"
    "v_add_f32 v160, 1.0, v160\n"
    "v_add_f32 v161, 1.0, v161\n"
    "v_add_f32 v162, 1.0, v162\n"
    "v_add_f32 v163, 1.0, v163\n"
    "v_add_f32 v164, 1.0, v164\n"
    "v_add_f32 v165, 1.0, v165\n"
    "v_add_f32 v166, 1.0, v166\n"
    "v_add_f32 v167, 1.0, v167\n"
    "v_rcp_f32 v160, v160\n"
    "v_rcp_f32 v161, v161\n"
    "v_rcp_f32 v162, v162\n"
    "v_rcp_f32 v163, v163\n"
    "v_rcp_f32 v164, v164\n"
    "v_rcp_f32 v165, v165\n"
    "v_rcp_f32 v166, v166\n"
    "v_rcp_f32 v167, v167\n"
    "v_mul_f32 v160, v64, v160\n"
    "v_mul_f32 v161, v65, v161\n"
    "v_mul_f32 v162, v66, v162\n"
    "v_mul_f32 v163, v67, v163\n"
    "v_mul_f32 v164, v80, v164\n"
    "v_mul_f32 v165, v81, v165\n"
    "v_mul_f32 v166, v82, v166\n"
    "v_mul_f32 v167, v83, v167\n"
    "v_cvt_pk_bf16_f32 v184, v160, v161\n"
    "v_cvt_pk_bf16_f32 v185, v162, v163\n"
    "v_cvt_pk_bf16_f32 v186, v164, v165\n"
    "v_cvt_pk_bf16_f32 v187, v166, v167\n"
    "s_add_u32 s52, s60, 16384\n"
    "buffer_store_dwordx4 v[184:187], v238, s[24:27], s52 offen offset:0 sc1\n"
    "ds_write_b128 v238, v[184:187] offset:16384\n"
    "v_fmac_f32 v68, v116, v144\n"
    "v_fmac_f32 v69, v116, v145\n"
    "v_fmac_f32 v70, v116, v146\n"
    "v_fmac_f32 v71, v116, v147\n"
    "v_fmac_f32 v84, v116, v148\n"
    "v_fmac_f32 v85, v116, v149\n"
    "v_fmac_f32 v86, v116, v150\n"
    "v_fmac_f32 v87, v116, v151\n"
    "v_mul_f32 v68, v119, v68\n"
    "v_mul_f32 v69, v119, v69\n"
    "v_mul_f32 v70, v119, v70\n"
    "v_mul_f32 v71, v119, v71\n"
    "v_mul_f32 v84, v119, v84\n"
    "v_mul_f32 v85, v119, v85\n"
    "v_mul_f32 v86, v119, v86\n"
    "v_mul_f32 v87, v119, v87\n"
    "v_exp_f32 v168, v68\n"
    "v_exp_f32 v169, v69\n"
    "v_exp_f32 v170, v70\n"
    "v_exp_f32 v171, v71\n"
    "v_exp_f32 v172, v84\n"
    "v_exp_f32 v173, v85\n"
    "v_exp_f32 v174, v86\n"
    "v_exp_f32 v175, v87\n"
    "v_add_f32 v168, 1.0, v168\n"
    "v_add_f32 v169, 1.0, v169\n"
    "v_add_f32 v170, 1.0, v170\n"
    "v_add_f32 v171, 1.0, v171\n"
    "v_add_f32 v172, 1.0, v172\n"
    "v_add_f32 v173, 1.0, v173\n"
    "v_add_f32 v174, 1.0, v174\n"
    "v_add_f32 v175, 1.0, v175\n"
    "v_rcp_f32 v168, v168\n"
    "v_rcp_f32 v169, v169\n"
    "v_rcp_f32 v170, v170\n"
    "v_rcp_f32 v171, v171\n"
    "v_rcp_f32 v172, v172\n"
    "v_rcp_f32 v173, v173\n"
    "v_rcp_f32 v174, v174\n"
    "v_rcp_f32 v175, v175\n"
    "v_mul_f32 v168, v68, v168\n"
    "v_mul_f32 v169, v69, v169\n"
    "v_mul_f32 v170, v70, v170\n"
    "v_mul_f32 v171, v71, v171\n"
    "v_mul_f32 v172, v84, v172\n"
    "v_mul_f32 v173, v85, v173\n"
    "v_mul_f32 v174, v86, v174\n"
    "v_mul_f32 v175, v87, v175\n"
    "v_cvt_pk_bf16_f32 v176, v168, v169\n"
    "v_cvt_pk_bf16_f32 v177, v170, v171\n"
    "v_cvt_pk_bf16_f32 v178, v172, v173\n"
    "v_cvt_pk_bf16_f32 v179, v174, v175\n"
    "buffer_store_dwordx4 v[176:179], v238, s[24:27], s52 offen offset:1024 sc1\n"
    "ds_write_b128 v238, v[176:179] offset:17408\n"
    "v_fmac_f32 v72, v120, v144\n"
    "v_fmac_f32 v73, v120, v145\n"
    "v_fmac_f32 v74, v120, v146\n"
    "v_fmac_f32 v75, v120, v147\n"
    "v_fmac_f32 v88, v120, v148\n"
    "v_fmac_f32 v89, v120, v149\n"
    "v_fmac_f32 v90, v120, v150\n"
    "v_fmac_f32 v91, v120, v151\n"
    "v_mul_f32 v72, v123, v72\n"
    "v_mul_f32 v73, v123, v73\n"
    "v_mul_f32 v74, v123, v74\n"
    "v_mul_f32 v75, v123, v75\n"
    "v_mul_f32 v88, v123, v88\n"
    "v_mul_f32 v89, v123, v89\n"
    "v_mul_f32 v90, v123, v90\n"
    "v_mul_f32 v91, v123, v91\n"
    "v_exp_f32 v160, v72\n"
    "v_exp_f32 v161, v73\n"
    "v_exp_f32 v162, v74\n"
    "v_exp_f32 v163, v75\n"
    "v_exp_f32 v164, v88\n"
    "v_exp_f32 v165, v89\n"
    "v_exp_f32 v166, v90\n"
    "v_exp_f32 v167, v91\n"
    "v_add_f32 v160, 1.0, v160\n"
    "v_add_f32 v161, 1.0, v161\n"
    "v_add_f32 v162, 1.0, v162\n"
    "v_add_f32 v163, 1.0, v163\n"
    "v_add_f32 v164, 1.0, v164\n"
    "v_add_f32 v165, 1.0, v165\n"
    "v_add_f32 v166, 1.0, v166\n"
    "v_add_f32 v167, 1.0, v167\n"
    "v_rcp_f32 v160, v160\n"
    "v_rcp_f32 v161, v161\n"
    "v_rcp_f32 v162, v162\n"
    "v_rcp_f32 v163, v163\n"
    "v_rcp_f32 v164, v164\n"
    "v_rcp_f32 v165, v165\n"
    "v_rcp_f32 v166, v166\n"
    "v_rcp_f32 v167, v167\n"
    "v_mul_f32 v160, v72, v160\n"
    "v_mul_f32 v161, v73, v161\n"
    "v_mul_f32 v162, v74, v162\n"
    "v_mul_f32 v163, v75, v163\n"
    "v_mul_f32 v164, v88, v164\n"
    "v_mul_f32 v165, v89, v165\n"
    "v_mul_f32 v166, v90, v166\n"
    "v_mul_f32 v167, v91, v167\n"
    "v_cvt_pk_bf16_f32 v180, v160, v161\n"
    "v_cvt_pk_bf16_f32 v181, v162, v163\n"
    "v_cvt_pk_bf16_f32 v182, v164, v165\n"
    "v_cvt_pk_bf16_f32 v183, v166, v167\n"
    "buffer_store_dwordx4 v[180:183], v239, s[24:27], s52 offen offset:0 sc1\n"
    "ds_write_b128 v239, v[180:183] offset:16384\n"
    "s_waitcnt vmcnt(3)\n"
    "v_mov_b32 v221, 1\n"
    "ds_write_b32 v219, v221 offset:32\n"
    "v_fmac_f32 v76, v124, v144\n"
    "v_fmac_f32 v77, v124, v145\n"
    "v_fmac_f32 v78, v124, v146\n"
    "v_fmac_f32 v79, v124, v147\n"
    "v_fmac_f32 v92, v124, v148\n"
    "v_fmac_f32 v93, v124, v149\n"
    "v_fmac_f32 v94, v124, v150\n"
    "v_fmac_f32 v95, v124, v151\n"
    "v_mul_f32 v76, v127, v76\n"
    "v_mul_f32 v77, v127, v77\n"
    "v_mul_f32 v78, v127, v78\n"
    "v_mul_f32 v79, v127, v79\n"
    "v_mul_f32 v92, v127, v92\n"
    "v_mul_f32 v93, v127, v93\n"
    "v_mul_f32 v94, v127, v94\n"
    "v_mul_f32 v95, v127, v95\n"
    "v_exp_f32 v168, v76\n"
    "v_exp_f32 v169, v77\n"
    "v_exp_f32 v170, v78\n"
    "v_exp_f32 v171, v79\n"
    "v_exp_f32 v172, v92\n"
    "v_exp_f32 v173, v93\n"
    "v_exp_f32 v174, v94\n"
    "v_exp_f32 v175, v95\n"
    "v_add_f32 v168, 1.0, v168\n"
    "v_add_f32 v169, 1.0, v169\n"
    "v_add_f32 v170, 1.0, v170\n"
    "v_add_f32 v171, 1.0, v171\n"
    "v_add_f32 v172, 1.0, v172\n"
    "v_add_f32 v173, 1.0, v173\n"
    "v_add_f32 v174, 1.0, v174\n"
    "v_add_f32 v175, 1.0, v175\n"
    "v_rcp_f32 v168, v168\n"
    "v_rcp_f32 v169, v169\n"
    "v_rcp_f32 v170, v170\n"
    "v_rcp_f32 v171, v171\n"
    "v_rcp_f32 v172, v172\n"
    "v_rcp_f32 v173, v173\n"
    "v_rcp_f32 v174, v174\n"
    "v_rcp_f32 v175, v175\n"
    "v_mul_f32 v168, v76, v168\n"
    "v_mul_f32 v169, v77, v169\n"
    "v_mul_f32 v170, v78, v170\n"
    "v_mul_f32 v171, v79, v171\n"
    "v_mul_f32 v172, v92, v172\n"
    "v_mul_f32 v173, v93, v173\n"
    "v_mul_f32 v174, v94, v174\n"
    "v_mul_f32 v175, v95, v175\n"
    "v_cvt_pk_bf16_f32 v184, v168, v169\n"
    "v_cvt_pk_bf16_f32 v185, v170, v171\n"
    "v_cvt_pk_bf16_f32 v186, v172, v173\n"
    "v_cvt_pk_bf16_f32 v187, v174, v175\n"
    "buffer_store_dwordx4 v[184:187], v239, s[24:27], s52 offen offset:1024 sc1\n"
    "ds_write_b128 v239, v[184:187] offset:17408\n"
    "s_cmp_lg_u32 s40, 2\n"
    "s_cbranch_scc1 Lep27_%=\n"
    "v_mov_b32 v244, 1\n"
    "Lw28_%=:\n"
    "ds_read_b32 v220, v218 offset:32\n"
    "s_waitcnt lgkmcnt(0)\n"
    "v_cmp_gt_u32 vcc, 1, v220\n"
    "s_cbranch_vccnz Ls29_%=\n"
    "s_mov_b64 exec, 15\n"
    "buffer_store_dwordx4 v[244:247], v222, s[24:27], s3 offen sc1\n"
    "s_mov_b64 exec, -1\n"
    "Lep27_%=:\n"
    "v_fmac_f32 v96, v112, v152\n"
    "v_fmac_f32 v97, v112, v153\n"
    "v_fmac_f32 v98, v112, v154\n"
    "v_fmac_f32 v99, v112, v155\n"
    "v_fmac_f32 v104, v112, v156\n"
    "v_fmac_f32 v105, v112, v157\n"
    "v_fmac_f32 v106, v112, v158\n"
    "v_fmac_f32 v107, v112, v159\n"
    "v_mul_f32 v96, v115, v96\n"
    "v_mul_f32 v97, v115, v97\n"
    "v_mul_f32 v98, v115, v98\n"
    "v_mul_f32 v99, v115, v99\n"
    "v_mul_f32 v104, v115, v104\n"
    "v_mul_f32 v105, v115, v105\n"
    "v_mul_f32 v106, v115, v106\n"
    "v_mul_f32 v107, v115, v107\n"
    "v_exp_f32 v160, v96\n"
    "v_exp_f32 v161, v97\n"
    "v_exp_f32 v162, v98\n"
    "v_exp_f32 v163, v99\n"
    "v_exp_f32 v164, v104\n"
    "v_exp_f32 v165, v105\n"
    "v_exp_f32 v166, v106\n"
    "v_exp_f32 v167, v107\n"
    "v_add_f32 v160, 1.0, v160\n"
    "v_add_f32 v161, 1.0, v161\n"
    "v_add_f32 v162, 1.0, v162\n"
    "v_add_f32 v163, 1.0, v163\n"
    "v_add_f32 v164, 1.0, v164\n"
    "v_add_f32 v165, 1.0, v165\n"
    "v_add_f32 v166, 1.0, v166\n"
    "v_add_f32 v167, 1.0, v167\n"
    "v_rcp_f32 v160, v160\n"
    "v_rcp_f32 v161, v161\n"
    "v_rcp_f32 v162, v162\n"
    "v_rcp_f32 v163, v163\n"
    "v_rcp_f32 v164, v164\n"
    "v_rcp_f32 v165, v165\n"
    "v_rcp_f32 v166, v166\n"
    "v_rcp_f32 v167, v167\n"
    "v_mul_f32 v160, v96, v160\n"
    "v_mul_f32 v161, v97, v161\n"
    "v_mul_f32 v162, v98, v162\n"
    "v_mul_f32 v163, v99, v163\n"
    "v_mul_f32 v164, v104, v164\n"
    "v_mul_f32 v165, v105, v165\n"
    "v_mul_f32 v166, v106, v166\n"
    "v_mul_f32 v167, v107, v167\n"
    "v_cvt_pk_bf16_f32 v176, v160, v161\n"
    "v_cvt_pk_bf16_f32 v177, v162, v163\n"
    "v_cvt_pk_bf16_f32 v178, v164, v165\n"
    "v_cvt_pk_bf16_f32 v179, v166, v167\n"
    "buffer_store_dwordx4 v[176:179], v229, s[24:27], s60 offen offset:0 sc1\n"
    "ds_write_b128 v229, v[176:179] offset:0\n"
    "v_fmac_f32 v100, v116, v152\n"
    "v_fmac_f32 v101, v116, v153\n"
    "v_fmac_f32 v102, v116, v154\n"
    "v_fmac_f32 v103, v116, v155\n"
    "v_fmac_f32 v108, v116, v156\n"
    "v_fmac_f32 v109, v116, v157\n"
    "v_fmac_f32 v110, v116, v158\n"
    "v_fmac_f32 v111, v116, v159\n"
    "v_mul_f32 v100, v119, v100\n"
    "v_mul_f32 v101, v119, v101\n"
    "v_mul_f32 v102, v119, v102\n"
    "v_mul_f32 v103, v119, v103\n"
    "v_mul_f32 v108, v119, v108\n"
    "v_mul_f32 v109, v119, v109\n"
    "v_mul_f32 v110, v119, v110\n"
    "v_mul_f32 v111, v119, v111\n"
    "v_exp_f32 v168, v100\n"
    "v_exp_f32 v169, v101\n"
    "v_exp_f32 v170, v102\n"
    "v_exp_f32 v171, v103\n"
    "v_exp_f32 v172, v108\n"
    "v_exp_f32 v173, v109\n"
    "v_exp_f32 v174, v110\n"
    "v_exp_f32 v175, v111\n"
    "v_add_f32 v168, 1.0, v168\n"
    "v_add_f32 v169, 1.0, v169\n"
    "v_add_f32 v170, 1.0, v170\n"
    "v_add_f32 v171, 1.0, v171\n"
    "v_add_f32 v172, 1.0, v172\n"
    "v_add_f32 v173, 1.0, v173\n"
    "v_add_f32 v174, 1.0, v174\n"
    "v_add_f32 v175, 1.0, v175\n"
    "v_rcp_f32 v168, v168\n"
    "v_rcp_f32 v169, v169\n"
    "v_rcp_f32 v170, v170\n"
    "v_rcp_f32 v171, v171\n"
    "v_rcp_f32 v172, v172\n"
    "v_rcp_f32 v173, v173\n"
    "v_rcp_f32 v174, v174\n"
    "v_rcp_f32 v175, v175\n"
    "v_mul_f32 v168, v100, v168\n"
    "v_mul_f32 v169, v101, v169\n"
    "v_mul_f32 v170, v102, v170\n"
    "v_mul_f32 v171, v103, v171\n"
    "v_mul_f32 v172, v108, v172\n"
    "v_mul_f32 v173, v109, v173\n"
    "v_mul_f32 v174, v110, v174\n"
    "v_mul_f32 v175, v111, v175\n"
    "v_cvt_pk_bf16_f32 v180, v168, v169\n"
    "v_cvt_pk_bf16_f32 v181, v170, v171\n"
    "v_cvt_pk_bf16_f32 v182, v172, v173\n"
    "v_cvt_pk_bf16_f32 v183, v174, v175\n"
    "buffer_store_dwordx4 v[180:183], v229, s[24:27], s60 offen offset:1024 sc1\n"
    "ds_write_b128 v229, v[180:183] offset:1024\n"
    "v_mov_b32 v221, 1\n"
    "ds_write_b32 v219, v221 offset:0\n"
    "s_lshl1_add_u32 s53, s41, s45\n"
    "s_lshl_b32 s53, s53, 10\n"
    "s_add_u32 s53, s53, 32768\n"
    "v_and_b32 v253, 15, v255\n"
    "v_lshlrev_b32 v253, 7, v253\n"
    "v_and_b32 v249, 0x3ff, v253\n"
    "v_mov_b32 v244, 2\n"
    "s_mul_i32 s50, s45, 56\n"
    "s_add_u32 s62, s62, s50\n"
    "s_lshl_b32 s62, s62, 13\n"
    "s_add_u32 s62, s62, 4718592\n"
    "s_lshl_b32 s50, s45, 7\n"
    "s_lshl_b32 s51, s45, 8\n"
    "s_sub_u32 s51, 128, s51\n"
    "s_lshl_b32 s61, s49, 16\n"
    "s_cmp_eq_u32 s49, 0\n"
    "s_cselect_b32 s62, s62, s50\n"
    "s_cselect_b32 s70, 13, 16\n"
    "s_cselect_b32 s71, 4096, s51\n"
    "s_cselect_b64 s[32:33], s[24:25], s[32:33]\n"
    "s_cselect_b32 s34, s26, s34\n"
    "s_cselect_b64 vcc, -1, 0\n"
    "v_cndmask_b32 v234, v234, v222, vcc\n"
    "v_cndmask_b32 v235, v235, v222, vcc\n"
    "v_cndmask_b32 v236, v236, v222, vcc\n"
    "v_cndmask_b32 v237, v237, v222, vcc\n"
    "s_cmp_lg_u32 s40, 2\n"
    "s_cbranch_scc1 Lpp30_%=\n"
    "s_cmp_eq_u32 s46, 1\n"
    "s_cselect_b32 s54, 0, 128\n"
    "v_mov_b32 v168, s54\n"
    "s_cmp_eq_u32 s46, 2\n"
    "s_cselect_b32 s54, 0, 256\n"
    "v_writelane_b32 v168, s54, 0\n"
    "buffer_load_dword v169, v168, s[24:27], s53 offen sc1\n"
    "Lpp30_%=:\n"
    "v_lshlrev_b32 v232, s70, v232\n"
    "v_add_u32 v232, s62, v232\n"
    "s_lshl_b32 s50, s48, 13\n"
    "s_add_u32 s61, s61, s50\n"
    "s_lshl4_add_u32 s50, s40, 135808\n"
    "v_lshl_add_u32 v160, v255, 6, s50\n"
    "v_lshrrev_b32 v248, 4, v255\n"
    "v_and_b32 v250, 15, v255\n"
    "s_lshr_b32 s50, s2, 3\n"
    "s_lshl2_add_u32 s50, s50, s40\n"
    "s_and_b32 s51, s2, 7\n"
    "s_lshl3_add_u32 s50, s50, s51\n"
    "s_lshl_b32 s50, s50, 10\n"
    "s_add_u32 s50, s50, 23592960\n"
    "v_add_u32 v161, s50, v222\n"
    "s_lshl_b32 s50, s49, 2\n"
    "v_or_b32 v251, s50, v248\n"
    "v_lshlrev_b32 v251, 1, v251\n"
    "v_xor_b32 v251, v251, v250\n"
    "v_lshlrev_b32 v251, 4, v251\n"
    "v_lshl_add_u32 v251, v248, 10, v251\n"
    "s_lshl1_add_u32 s51, s46, s45\n"
    "s_lshr_b32 s50, s41, 2\n"
    "s_lshl4_add_u32 s50, s50, s51\n"
    "s_lshl2_add_u32 s50, s50, s40\n"
    "s_lshl_b32 s50, s50, 6\n"
    "s_and_b32 s52, s41, 3\n"
    "s_add_u32 s50, s50, s52\n"
    "s_lshl_b32 s50, s50, 8\n"
    "s_add_u32 s50, s50, 23592960\n"
    "v_add_u32 v162, s50, v251\n"
    "s_lshl_b32 s58, s51, 11\n"
    "s_add_u32 s58, s58, 65536\n"
    "v_add_u32 v253, s58, v253\n"
    "s_xor_b32 s58, s43, 512\n"
    "v_writelane_b32 v253, s58, 63\n"
    "s_lshl_b32 s58, s40, 28\n"
    "v_add_u32 v253, s58, v253\n"
    "v_add_u32 v163, s58, v222\n"
    "v_and_b32 v250, 7, v255\n"
    "v_xor_b32 v250, v250, v248\n"
    "v_lshlrev_b32 v250, 4, v250\n"
    "v_lshrrev_b32 v251, 3, v255\n"
    "v_lshl_add_u32 v164, v251, 11, v250\n"
    "v_xor_b32 v165, 64, v164\n"
    "v_add_u32 v166, 30720, v164\n"
    "v_add_u32 v167, 46080, v165\n"
    "v_add_u32 v165, 15360, v165\n"
    "s_lshl2_add_u32 s59, s41, s40\n"
    "s_lshl_b32 s59, s59, 16\n"
    "s_lshl_b32 s51, s51, 7\n"
    "s_add_u32 s59, s59, s51\n"
    "s_lshl_b32 s57, s40, 12\n"
    "s_waitcnt vmcnt(0)\n"
    "ds_write_b32 v219, v244 offset:32\n"
    "s_cmp_lg_u32 s40, 0\n"
    "s_cbranch_scc1 Lfp32_%=\n"
    "Lw37_%=:\n"
    "ds_read_b32 v220, v218 offset:32\n"
    "s_waitcnt lgkmcnt(0)\n"
    "v_cmp_gt_u32 vcc, 2, v220\n"
    "s_cbranch_vccnz Ls38_%=\n"
    "s_mov_b64 exec, 15\n"
    "buffer_store_dwordx4 v[244:247], v222, s[24:27], s3 offen sc1\n"
    "s_mov_b64 exec, -1\n"
    "Lfp32_%=:\n"
    "s_cmp_lg_u32 s40, 2\n"
    "s_cbranch_scc1 Lb34_%=\n"
    "v_cmp_gt_u32 vcc, 1, v169\n"
    "s_cbranch_vccnz Lh33_%=\n"
    "Lb34_%=:\n"
    "s_mov_b32 s44, s53\n"
    "s_xor_b32 s54, s40, 2\n"
    "s_lshl_b32 s54, s54, 28\n"
    "v_or_b32 v249, s54, v249\n"
    "s_cmp_lg_u32 s40, 2\n"
    "s_cbranch_scc1 La739_%=\n"
    "Lw40_%=:\n"
    "ds_read_b32 v220, v218 offset:16\n"
    "s_waitcnt lgkmcnt(0)\n"
    "v_cmp_gt_u32 vcc, 1, v220\n"
    "s_cbranch_vccnz Ls41_%=\n"
    "s_add_u32 m0, s61, 49152\n"
    "v_readlane_b32 s50, v232, 3\n"
    "s_nop 0\n"
    "s_mov_b32 s51, s50\n"
    "buffer_load_dwordx4 v234, s[32:35], s51 offen sc1 lds\n"
    "buffer_load_dwordx4 v235, s[32:35], s51 offen offset:1024 sc1 lds\n"
    "buffer_load_dwordx4 v236, s[32:35], s51 offen offset:2048 sc1 lds\n"
    "buffer_load_dwordx4 v237, s[32:35], s51 offen offset:3072 sc1 lds\n"
    "s_add_u32 m0, s61, 53248\n"
    "s_add_u32 s51, s51, s71\n"
    "buffer_load_dwordx4 v234, s[32:35], s51 offen sc1 lds\n"
    "buffer_load_dwordx4 v235, s[32:35], s51 offen offset:1024 sc1 lds\n"
    "buffer_load_dwordx4 v236, s[32:35], s51 offen offset:2048 sc1 lds\n"
    "buffer_load_dwordx4 v237, s[32:35], s51 offen offset:3072 sc1 lds\n"
    "La739_%=:\n"
    "s_lshl4_add_u32 s53, s41, s42\n"
    "s_lshl2_add_u32 s53, s53, s40\n"
    "s_lshl_b32 s53, s53, 12\n"
    "s_add_u32 s53, s53, 19398656\n"
    "v_mov_b32 v16, -1\n"
    "v_mov_b32 v17, -1\n"
    "v_mov_b32 v18, -1\n"
    "v_mov_b32 v19, -1\n"
    "buffer_store_dwordx4 v[16:19], v222, s[24:27], s53 offen offset:0 sc1\n"
    "buffer_store_dwordx4 v[16:19], v222, s[24:27], s53 offen offset:1024 sc1\n"
    "buffer_store_dwordx4 v[16:19], v222, s[24:27], s53 offen offset:2048 sc1\n"
    "buffer_store_dwordx4 v[16:19], v222, s[24:27], s53 offen offset:3072 sc1\n"
    "s_cmp_eq_u32 s40, 2\n"
    "s_cbranch_scc1 Lyq46_%=\n"
    "s_min_u32 s53, s40, 2\n"
    "s_lshl_b32 s54, s53, 2\n"
    "s_lshr_b32 s54, 515, s54\n"
    "s_and_b32 s54, s54, 3\n"
    "s_add_u32 s53, s53, 1\n"
    "s_lshl_b32 s55, s54, 12\n"
    "s_add_u32 s55, s55, 135808\n"
    "v_lshl_add_u32 v248, v255, 6, s55\n"
    "s_lshl_b32 s54, s54, 20\n"
    "s_lshl_b32 s55, s40, 13\n"
    "v_subrev_u32 v251, s55, v161\n"
    "Lw47_%=:\n"
    "ds_read_b32 v220, v218 offset:48\n"
    "s_waitcnt lgkmcnt(0)\n"
    "v_cmp_gt_u32 vcc, s53, v220\n"
    "s_cbranch_vccnz Ls48_%=\n"
    "ds_read_b128 v[0:3], v248 offset:0\n"
    "ds_read_b128 v[4:7], v248 offset:16\n"
    "ds_read_b128 v[8:11], v248 offset:32\n"
    "ds_read_b128 v[12:15], v248 offset:48\n"
    "s_waitcnt lgkmcnt(3)\n"
    "buffer_store_dwordx4 v[0:3], v251, s[24:27], s54 offen sc1\n"
    "s_add_u32 s54, s54, 8192\n"
    "s_waitcnt lgkmcnt(2)\n"
    "buffer_store_dwordx4 v[4:7], v251, s[24:27], s54 offen sc1\n"
    "s_add_u32 s54, s54, 8192\n"
    "s_waitcnt lgkmcnt(1)\n"
    "buffer_store_dwordx4 v[8:11], v251, s[24:27], s54 offen sc1\n"
    "s_add_u32 s54, s54, 8192\n"
    "s_waitcnt lgkmcnt(0)\n"
    "buffer_store_dwordx4 v[12:15], v251, s[24:27], s54 offen sc1\n"
    "s_add_u32 s54, s54, 8192\n"
    "Lyq46_%=:\n"
    "v_readlane_b32 s50, v232, 4\n"
    "s_nop 0\n"
    "s_mov_b32 s51, s50\n"
    "s_add_u32 m0, s61, 0\n"
    "s_barrier\n"
    "buffer_load_dwordx4 v234, s[32:35], s51 offen sc1 lds\n"
    "buffer_load_dwordx4 v235, s[32:35], s51 offen offset:1024 sc1 lds\n"
    "buffer_load_dwordx4 v236, s[32:35], s51 offen offset:2048 sc1 lds\n"
    "buffer_load_dwordx4 v237, s[32:35], s51 offen offset:3072 sc1 lds\n"
    "s_add_u32 m0, s61, 4096\n"
    "s_add_u32 s51, s51, s71\n"
    "buffer_load_dwordx4 v234, s[32:35], s51 offen sc1 lds\n"
    "buffer_load_dwordx4 v235, s[32:35], s51 offen offset:1024 sc1 lds\n"
    "buffer_load_dwordx4 v236, s[32:35], s51 offen offset:2048 sc1 lds\n"
    "buffer_load_dwordx4 v237, s[32:35], s51 offen offset:3072 sc1 lds\n"
    "v_readlane_b32 s50, v232, 5\n"
    "s_nop 0\n"
    "s_mov_b32 s51, s50\n"
    "s_add_u32 m0, s61, 16384\n"
    "s_barrier\n"
    "buffer_load_dwordx4 v234, s[32:35], s51 offen sc1 lds\n"
    "buffer_load_dwordx4 v235, s[32:35], s51 offen offset:1024 sc1 lds\n"
    "buffer_load_dwordx4 v236, s[32:35], s51 offen offset:2048 sc1 lds\n"
    "buffer_load_dwordx4 v237, s[32:35], s51 offen offset:3072 sc1 lds\n"
    "s_add_u32 m0, s61, 20480\n"
    "s_add_u32 s51, s51, s71\n"
    "buffer_load_dwordx4 v234, s[32:35], s51 offen sc1 lds\n"
    "buffer_load_dwordx4 v235, s[32:35], s51 offen offset:1024 sc1 lds\n"
    "buffer_load_dwordx4 v236, s[32:35], s51 offen offset:2048 sc1 lds\n"
    "buffer_load_dwordx4 v237, s[32:35], s51 offen offset:3072 sc1 lds\n"
    "buffer_load_dword v250, v249, s[24:27], s44 offen sc1\n"
    "ds_read_b128 v[20:23], v160 offset:4096\n"
    "v_readlane_b32 s50, v232, 6\n"
    "s_nop 0\n"
    "s_mov_b32 s51, s50\n"
    "s_add_u32 m0, s61, 32768\n"
    "s_waitcnt vmcnt(21)\n"
    "s_barrier\n"
    "buffer_load_dwordx4 v234, s[32:35], s51 offen sc1 lds\n"
    "buffer_load_dwordx4 v235, s[32:35], s51 offen offset:1024 sc1 lds\n"
    "buffer_load_dwordx4 v236, s[32:35], s51 offen offset:2048 sc1 lds\n"
    "buffer_load_dwordx4 v237, s[32:35], s51 offen offset:3072 sc1 lds\n"
    "s_add_u32 m0, s61, 36864\n"
    "s_add_u32 s51, s51, s71\n"
    "buffer_load_dwordx4 v234, s[32:35], s51 offen sc1 lds\n"
    "buffer_load_dwordx4 v235, s[32:35], s51 offen offset:1024 sc1 lds\n"
    "buffer_load_dwordx4 v236, s[32:35], s51 offen offset:2048 sc1 lds\n"
    "buffer_load_dwordx4 v237, s[32:35], s51 offen offset:3072 sc1 lds\n"
    "s_waitcnt vmcnt(8)\n"
    "s_cmp_lg_u32 s40, 2\n"
    "s_cbranch_scc1 Lb43_%=\n"
    "v_cmp_gt_u32 vcc, 2, v250\n"
    "s_cbranch_vccnz Lh42_%=\n"
    "Lb43_%=:\n"
    "s_waitcnt lgkmcnt(0)\n"
    "s_mov_b32 s53, 1048576\n"
    "buffer_store_dwordx4 v[20:23], v161, s[24:27], s53 offen sc1\n"
    "v_readlane_b32 s50, v232, 7\n"
    "s_nop 0\n"
    "s_mov_b32 s51, s50\n"
    "s_add_u32 m0, s61, 49152\n"
    "s_barrier\n"
    "buffer_load_dwordx4 v234, s[32:35], s51 offen sc1 lds\n"
    "buffer_load_dwordx4 v235, s[32:35], s51 offen offset:1024 sc1 lds\n"
    "buffer_load_dwordx4 v236, s[32:35], s51 offen offset:2048 sc1 lds\n"
    "buffer_load_dwordx4 v237, s[32:35], s51 offen offset:3072 sc1 lds\n"
    "s_add_u32 m0, s61, 53248\n"
    "s_add_u32 s51, s51, s71\n"
    "buffer_load_dwordx4 v234, s[32:35], s51 offen sc1 lds\n"
    "buffer_load_dwordx4 v235, s[32:35], s51 offen offset:1024 sc1 lds\n"
    "buffer_load_dwordx4 v236, s[32:35], s51 offen offset:2048 sc1 lds\n"
    "buffer_load_dwordx4 v237, s[32:35], s51 offen offset:3072 sc1 lds\n"
    "v_readlane_b32 s50, v232, 8\n"
    "s_nop 0\n"
    "s_mov_b32 s51, s50\n"
    "s_add_u32 m0, s61, 0\n"
    "s_barrier\n"
    "buffer_load_dwordx4 v234, s[32:35], s51 offen sc1 lds\n"
    "buffer_load_dwordx4 v235, s[32:35], s51 offen offset:1024 sc1 lds\n"
    "buffer_load_dwordx4 v236, s[32:35], s51 offen offset:2048 sc1 lds\n"
    "buffer_load_dwordx4 v237, s[32:35], s51 offen offset:3072 sc1 lds\n"
    "s_add_u32 m0, s61, 4096\n"
    "s_add_u32 s51, s51, s71\n"
    "buffer_load_dwordx4 v234, s[32:35], s51 offen sc1 lds\n"
    "buffer_load_dwordx4 v235, s[32:35], s51 offen offset:1024 sc1 lds\n"
    "buffer_load_dwordx4 v236, s[32:35], s51 offen offset:2048 sc1 lds\n"
    "buffer_load_dwordx4 v237, s[32:35], s51 offen offset:3072 sc1 lds\n"
    "v_readlane_b32 s50, v232, 9\n"
    "s_nop 0\n"
    "s_mov_b32 s51, s50\n"
    "s_add_u32 m0, s61, 16384\n"
    "s_waitcnt vmcnt(17)\n"
    "s_barrier\n"
    "buffer_load_dwordx4 v234, s[32:35], s51 offen sc1 lds\n"
    "buffer_load_dwordx4 v235, s[32:35], s51 offen offset:1024 sc1 lds\n"
    "buffer_load_dwordx4 v236, s[32:35], s51 offen offset:2048 sc1 lds\n"
    "buffer_load_dwordx4 v237, s[32:35], s51 offen offset:3072 sc1 lds\n"
    "s_add_u32 m0, s61, 20480\n"
    "s_add_u32 s51, s51, s71\n"
    "buffer_load_dwordx4 v234, s[32:35], s51 offen sc1 lds\n"
    "buffer_load_dwordx4 v235, s[32:35], s51 offen offset:1024 sc1 lds\n"
    "buffer_load_dwordx4 v236, s[32:35], s51 offen offset:2048 sc1 lds\n"
    "buffer_load_dwordx4 v237, s[32:35], s51 offen offset:3072 sc1 lds\n"
    "v_readlane_b32 s50, v232, 10\n"
    "s_nop 0\n"
    "s_mov_b32 s51, s50\n"
    "s_add_u32 m0, s61, 32768\n"
    "s_waitcnt vmcnt(16)\n"
    "s_barrier\n"
    "buffer_load_dwordx4 v234, s[32:35], s51 offen sc1 lds\n"
    "buffer_load_dwordx4 v235, s[32:35], s51 offen offset:1024 sc1 lds\n"
    "buffer_load_dwordx4 v236, s[32:35], s51 offen offset:2048 sc1 lds\n"
    "buffer_load_dwordx4 v237, s[32:35], s51 offen offset:3072 sc1 lds\n"
    "s_add_u32 m0, s61, 36864\n"
    "s_add_u32 s51, s51, s71\n"
    "buffer_load_dwordx4 v234, s[32:35], s51 offen sc1 lds\n"
    "buffer_load_dwordx4 v235, s[32:35], s51 offen offset:1024 sc1 lds\n"
    "buffer_load_dwordx4 v236, s[32:35], s51 offen offset:2048 sc1 lds\n"
    "buffer_load_dwordx4 v237, s[32:35], s51 offen offset:3072 sc1 lds\n"
    "v_readlane_b32 s50, v232, 11\n"
    "s_nop 0\n"
    "s_mov_b32 s51, s50\n"
    "s_add_u32 m0, s61, 49152\n"
    "s_waitcnt vmcnt(16)\n"
    "s_barrier\n"
    "buffer_load_dwordx4 v234, s[32:35], s51 offen sc1 lds\n"
    "buffer_load_dwordx4 v235, s[32:35], s51 offen offset:1024 sc1 lds\n"
    "buffer_load_dwordx4 v236, s[32:35], s51 offen offset:2048 sc1 lds\n"
    "buffer_load_dwordx4 v237, s[32:35], s51 offen offset:3072 sc1 lds\n"
    "s_add_u32 m0, s61, 53248\n"
    "s_add_u32 s51, s51, s71\n"
    "buffer_load_dwordx4 v234, s[32:35], s51 offen sc1 lds\n"
    "buffer_load_dwordx4 v235, s[32:35], s51 offen offset:1024 sc1 lds\n"
    "buffer_load_dwordx4 v236, s[32:35], s51 offen offset:2048 sc1 lds\n"
    "buffer_load_dwordx4 v237, s[32:35], s51 offen offset:3072 sc1 lds\n"
    "s_load_dword s68, s[22:23], s59\n"
    "v_readlane_b32 s50, v232, 12\n"
    "s_nop 0\n"
    "s_mov_b32 s51, s50\n"
    "s_add_u32 m0, s61, 0\n"
    "s_waitcnt vmcnt(16)\n"
    "s_barrier\n"
    "buffer_load_dwordx4 v234, s[32:35], s51 offen sc1 lds\n"
    "buffer_load_dwordx4 v235, s[32:35], s51 offen offset:1024 sc1 lds\n"
    "buffer_load_dwordx4 v236, s[32:35], s51 offen offset:2048 sc1 lds\n"
    "buffer_load_dwordx4 v237, s[32:35], s51 offen offset:3072 sc1 lds\n"
    "s_add_u32 m0, s61, 4096\n"
    "s_add_u32 s51, s51, s71\n"
    "buffer_load_dwordx4 v234, s[32:35], s51 offen sc1 lds\n"
    "buffer_load_dwordx4 v235, s[32:35], s51 offen offset:1024 sc1 lds\n"
    "buffer_load_dwordx4 v236, s[32:35], s51 offen offset:2048 sc1 lds\n"
    "buffer_load_dwordx4 v237, s[32:35], s51 offen offset:3072 sc1 lds\n"
    "v_readlane_b32 s50, v232, 13\n"
    "s_nop 0\n"
    "s_mov_b32 s51, s50\n"
    "s_add_u32 m0, s61, 16384\n"
    "s_waitcnt vmcnt(16)\n"
    "s_barrier\n"
    "buffer_load_dwordx4 v234, s[32:35], s51 offen sc1 lds\n"
    "buffer_load_dwordx4 v235, s[32:35], s51 offen offset:1024 sc1 lds\n"
    "buffer_load_dwordx4 v236, s[32:35], s51 offen offset:2048 sc1 lds\n"
    "buffer_load_dwordx4 v237, s[32:35], s51 offen offset:3072 sc1 lds\n"
    "s_add_u32 m0, s61, 20480\n"
    "s_add_u32 s51, s51, s71\n"
    "buffer_load_dwordx4 v234, s[32:35], s51 offen sc1 lds\n"
    "buffer_load_dwordx4 v235, s[32:35], s51 offen offset:1024 sc1 lds\n"
    "buffer_load_dwordx4 v236, s[32:35], s51 offen offset:2048 sc1 lds\n"
    "buffer_load_dwordx4 v237, s[32:35], s51 offen offset:3072 sc1 lds\n"
    "s_mov_b64 exec, 15\n"
    "buffer_store_dwordx4 v[244:247], v163, s[24:27], s43 offen sc1\n"
    "s_mov_b64 exec, -1\n"
    "v_readlane_b32 s50, v232, 14\n"
    "s_nop 0\n"
    "s_mov_b32 s51, s50\n"
    "s_add_u32 m0, s61, 32768\n"
    "s_waitcnt vmcnt(17)\n"
    "s_barrier\n"
    "buffer_load_dwordx4 v234, s[32:35], s51 offen sc1 lds\n"
    "buffer_load_dwordx4 v235, s[32:35], s51 offen offset:1024 sc1 lds\n"
    "buffer_load_dwordx4 v236, s[32:35], s51 offen offset:2048 sc1 lds\n"
    "buffer_load_dwordx4 v237, s[32:35], s51 offen offset:3072 sc1 lds\n"
    "s_add_u32 m0, s61, 36864\n"
    "s_add_u32 s51, s51, s71\n"
    "buffer_load_dwordx4 v234, s[32:35], s51 offen sc1 lds\n"
    "buffer_load_dwordx4 v235, s[32:35], s51 offen offset:1024 sc1 lds\n"
    "buffer_load_dwordx4 v236, s[32:35], s51 offen offset:2048 sc1 lds\n"
    "buffer_load_dwordx4 v237, s[32:35], s51 offen offset:3072 sc1 lds\n"
    "v_readlane_b32 s50, v232, 15\n"
    "s_nop 0\n"
    "s_mov_b32 s51, s50\n"
    "s_add_u32 m0, s61, 49152\n"
    "s_waitcnt vmcnt(17)\n"
    "s_barrier\n"
    "buffer_load_dwordx4 v234, s[32:35], s51 offen sc1 lds\n"
    "buffer_load_dwordx4 v235, s[32:35], s51 offen offset:1024 sc1 lds\n"
    "buffer_load_dwordx4 v236, s[32:35], s51 offen offset:2048 sc1 lds\n"
    "buffer_load_dwordx4 v237, s[32:35], s51 offen offset:3072 sc1 lds\n"
    "s_add_u32 m0, s61, 53248\n"
    "s_add_u32 s51, s51, s71\n"
    "buffer_load_dwordx4 v234, s[32:35], s51 offen sc1 lds\n"
    "buffer_load_dwordx4 v235, s[32:35], s51 offen offset:1024 sc1 lds\n"
    "buffer_load_dwordx4 v236, s[32:35], s51 offen offset:2048 sc1 lds\n"
    "buffer_load_dwordx4 v237, s[32:35], s51 offen offset:3072 sc1 lds\n"
    "v_readlane_b32 s50, v232, 16\n"
    "s_nop 0\n"
    "s_mov_b32 s51, s50\n"
    "s_add_u32 m0, s61, 0\n"
    "s_waitcnt vmcnt(17)\n"
    "s_barrier\n"
    "buffer_load_dwordx4 v234, s[32:35], s51 offen sc1 lds\n"
    "buffer_load_dwordx4 v235, s[32:35], s51 offen offset:1024 sc1 lds\n"
    "buffer_load_dwordx4 v236, s[32:35], s51 offen offset:2048 sc1 lds\n"
    "buffer_load_dwordx4 v237, s[32:35], s51 offen offset:3072 sc1 lds\n"
    "s_add_u32 m0, s61, 4096\n"
    "s_add_u32 s51, s51, s71\n"
    "buffer_load_dwordx4 v234, s[32:35], s51 offen sc1 lds\n"
    "buffer_load_dwordx4 v235, s[32:35], s51 offen offset:1024 sc1 lds\n"
    "buffer_load_dwordx4 v236, s[32:35], s51 offen offset:2048 sc1 lds\n"
    "buffer_load_dwordx4 v237, s[32:35], s51 offen offset:3072 sc1 lds\n"
    "v_readlane_b32 s50, v232, 17\n"
    "s_nop 0\n"
    "s_mov_b32 s51, s50\n"
    "s_add_u32 m0, s61, 16384\n"
    "s_waitcnt vmcnt(16)\n"
    "s_barrier\n"
    "buffer_load_dwordx4 v234, s[32:35], s51 offen sc1 lds\n"
    "buffer_load_dwordx4 v235, s[32:35], s51 offen offset:1024 sc1 lds\n"
    "buffer_load_dwordx4 v236, s[32:35], s51 offen offset:2048 sc1 lds\n"
    "buffer_load_dwordx4 v237, s[32:35], s51 offen offset:3072 sc1 lds\n"
    "s_add_u32 m0, s61, 20480\n"
    "s_add_u32 s51, s51, s71\n"
    "buffer_load_dwordx4 v234, s[32:35], s51 offen sc1 lds\n"
    "buffer_load_dwordx4 v235, s[32:35], s51 offen offset:1024 sc1 lds\n"
    "buffer_load_dwordx4 v236, s[32:35], s51 offen offset:2048 sc1 lds\n"
    "buffer_load_dwordx4 v237, s[32:35], s51 offen offset:3072 sc1 lds\n"
    "v_readlane_b32 s50, v232, 18\n"
    "s_nop 0\n"
    "s_mov_b32 s51, s50\n"
    "s_add_u32 m0, s61, 32768\n"
    "s_waitcnt vmcnt(16)\n"
    "s_barrier\n"
    "buffer_load_dwordx4 v234, s[32:35], s51 offen sc1 lds\n"
    "buffer_load_dwordx4 v235, s[32:35], s51 offen offset:1024 sc1 lds\n"
    "buffer_load_dwordx4 v236, s[32:35], s51 offen offset:2048 sc1 lds\n"
    "buffer_load_dwordx4 v237, s[32:35], s51 offen offset:3072 sc1 lds\n"
    "s_add_u32 m0, s61, 36864\n"
    "s_add_u32 s51, s51, s71\n"
    "buffer_load_dwordx4 v234, s[32:35], s51 offen sc1 lds\n"
    "buffer_load_dwordx4 v235, s[32:35], s51 offen offset:1024 sc1 lds\n"
    "buffer_load_dwordx4 v236, s[32:35], s51 offen offset:2048 sc1 lds\n"
    "buffer_load_dwordx4 v237, s[32:35], s51 offen offset:3072 sc1 lds\n"
    "v_readlane_b32 s50, v232, 19\n"
    "s_nop 0\n"
    "s_mov_b32 s51, s50\n"
    "s_add_u32 m0, s61, 49152\n"
    "s_waitcnt vmcnt(16)\n"
    "s_barrier\n"
    "buffer_load_dwordx4 v234, s[32:35], s51 offen sc1 lds\n"
    "buffer_load_dwordx4 v235, s[32:35], s51 offen offset:1024 sc1 lds\n"
    "buffer_load_dwordx4 v236, s[32:35], s51 offen offset:2048 sc1 lds\n"
    "buffer_load_dwordx4 v237, s[32:35], s51 offen offset:3072 sc1 lds\n"
    "s_add_u32 m0, s61, 53248\n"
    "s_add_u32 s51, s51, s71\n"
    "buffer_load_dwordx4 v234, s[32:35], s51 offen sc1 lds\n"
    "buffer_load_dwordx4 v235, s[32:35], s51 offen offset:1024 sc1 lds\n"
    "buffer_load_dwordx4 v236, s[32:35], s51 offen offset:2048 sc1 lds\n"
    "buffer_load_dwordx4 v237, s[32:35], s51 offen offset:3072 sc1 lds\n"
    "v_readlane_b32 s50, v232, 20\n"
    "s_nop 0\n"
    "s_mov_b32 s51, s50\n"
    "s_add_u32 m0, s61, 0\n"
    "s_waitcnt vmcnt(16)\n"
    "s_barrier\n"
    "buffer_load_dwordx4 v234, s[32:35], s51 offen sc1 lds\n"
    "buffer_load_dwordx4 v235, s[32:35], s51 offen offset:1024 sc1 lds\n"
    "buffer_load_dwordx4 v236, s[32:35], s51 offen offset:2048 sc1 lds\n"
    "buffer_load_dwordx4 v237, s[32:35], s51 offen offset:3072 sc1 lds\n"
    "s_add_u32 m0, s61, 4096\n"
    "s_add_u32 s51, s51, s71\n"
    "buffer_load_dwordx4 v234, s[32:35], s51 offen sc1 lds\n"
    "buffer_load_dwordx4 v235, s[32:35], s51 offen offset:1024 sc1 lds\n"
    "buffer_load_dwordx4 v236, s[32:35], s51 offen offset:2048 sc1 lds\n"
    "buffer_load_dwordx4 v237, s[32:35], s51 offen offset:3072 sc1 lds\n"
    "buffer_load_dword v252, v253, s[24:27], 0 offen sc1\n"
    "v_readlane_b32 s50, v232, 21\n"
    "s_nop 0\n"
    "s_mov_b32 s51, s50\n"
    "s_add_u32 m0, s61, 16384\n"
    "s_waitcnt vmcnt(17)\n"
    "s_barrier\n"
    "buffer_load_dwordx4 v234, s[32:35], s51 offen sc1 lds\n"
    "buffer_load_dwordx4 v235, s[32:35], s51 offen offset:1024 sc1 lds\n"
    "buffer_load_dwordx4 v236, s[32:35], s51 offen offset:2048 sc1 lds\n"
    "buffer_load_dwordx4 v237, s[32:35], s51 offen offset:3072 sc1 lds\n"
    "s_add_u32 m0, s61, 20480\n"
    "s_add_u32 s51, s51, s71\n"
    "buffer_load_dwordx4 v234, s[32:35], s51 offen sc1 lds\n"
    "buffer_load_dwordx4 v235, s[32:35], s51 offen offset:1024 sc1 lds\n"
    "buffer_load_dwordx4 v236, s[32:35], s51 offen offset:2048 sc1 lds\n"
    "buffer_load_dwordx4 v237, s[32:35], s51 offen offset:3072 sc1 lds\n"
    "v_readlane_b32 s50, v232, 22\n"
    "s_nop 0\n"
    "s_mov_b32 s51, s50\n"
    "s_add_u32 m0, s61, 32768\n"
    "s_waitcnt vmcnt(17)\n"
    "s_barrier\n"
    "buffer_load_dwordx4 v234, s[32:35], s51 offen sc1 lds\n"
    "buffer_load_dwordx4 v235, s[32:35], s51 offen offset:1024 sc1 lds\n"
    "buffer_load_dwordx4 v236, s[32:35], s51 offen offset:2048 sc1 lds\n"
    "buffer_load_dwordx4 v237, s[32:35], s51 offen offset:3072 sc1 lds\n"
    "s_add_u32 m0, s61, 36864\n"
    "s_add_u32 s51, s51, s71\n"
    "buffer_load_dwordx4 v234, s[32:35], s51 offen sc1 lds\n"
    "buffer_load_dwordx4 v235, s[32:35], s51 offen offset:1024 sc1 lds\n"
    "buffer_load_dwordx4 v236, s[32:35], s51 offen offset:2048 sc1 lds\n"
    "buffer_load_dwordx4 v237, s[32:35], s51 offen offset:3072 sc1 lds\n"
    "s_waitcnt vmcnt(16)\n"
    "s_cmp_lg_u32 s40, 0\n"
    "s_cbranch_scc1 Lb50_%=\n"
    "v_cmp_gt_u32 vcc, 1, v252\n"
    "s_cbranch_vccnz Lh49_%=\n"
    "Lb50_%=:\n"
    "v_readlane_b32 s50, v232, 23\n"
    "s_nop 0\n"
    "s_mov_b32 s51, s50\n"
    "s_add_u32 m0, s61, 49152\n"
    "s_barrier\n"
    "buffer_load_dwordx4 v234, s[32:35], s51 offen sc1 lds\n"
    "buffer_load_dwordx4 v235, s[32:35], s51 offen offset:1024 sc1 lds\n"
    "buffer_load_dwordx4 v236, s[32:35], s51 offen offset:2048 sc1 lds\n"
    "buffer_load_dwordx4 v237, s[32:35], s51 offen offset:3072 sc1 lds\n"
    "s_add_u32 m0, s61, 53248\n"
    "s_add_u32 s51, s51, s71\n"
    "buffer_load_dwordx4 v234, s[32:35], s51 offen sc1 lds\n"
    "buffer_load_dwordx4 v235, s[32:35], s51 offen offset:1024 sc1 lds\n"
    "buffer_load_dwordx4 v236, s[32:35], s51 offen offset:2048 sc1 lds\n"
    "buffer_load_dwordx4 v237, s[32:35], s51 offen offset:3072 sc1 lds\n"
    "s_add_u32 m0, s57, 135424\n"
    "s_nop 0\n"
    "s_mov_b32 s53, 0\n"
    "buffer_load_dwordx4 v162, s[24:27], s53 offen sc1 lds\n"
    "s_mov_b32 s53, 3072\n"
    "buffer_load_dwordx4 v162, s[24:27], s53 offen offset:1024 sc1 lds\n"
    "s_mov_b32 s53, 6144\n"
    "buffer_load_dwordx4 v162, s[24:27], s53 offen offset:2048 sc1 lds\n"
    "s_mov_b32 s53, 9216\n"
    "buffer_load_dwordx4 v162, s[24:27], s53 offen offset:3072 sc1 lds\n"
    "v_readlane_b32 s50, v232, 24\n"
    "s_nop 0\n"
    "s_mov_b32 s51, s50\n"
    "s_add_u32 m0, s61, 0\n"
    "s_waitcnt vmcnt(20)\n"
    "s_barrier\n"
    "buffer_load_dwordx4 v234, s[32:35], s51 offen sc1 lds\n"
    "buffer_load_dwordx4 v235, s[32:35], s51 offen offset:1024 sc1 lds\n"
    "buffer_load_dwordx4 v236, s[32:35], s51 offen offset:2048 sc1 lds\n"
    "buffer_load_dwordx4 v237, s[32:35], s51 offen offset:3072 sc1 lds\n"
    "s_add_u32 m0, s61, 4096\n"
    "s_add_u32 s51, s51, s71\n"
    "buffer_load_dwordx4 v234, s[32:35], s51 offen sc1 lds\n"
    "buffer_load_dwordx4 v235, s[32:35], s51 offen offset:1024 sc1 lds\n"
    "buffer_load_dwordx4 v236, s[32:35], s51 offen offset:2048 sc1 lds\n"
    "buffer_load_dwordx4 v237, s[32:35], s51 offen offset:3072 sc1 lds\n"
    "v_readlane_b32 s50, v232, 25\n"
    "s_nop 0\n"
    "s_mov_b32 s51, s50\n"
    "s_add_u32 m0, s61, 16384\n"
    "s_waitcnt vmcnt(20)\n"
    "s_barrier\n"
    "buffer_load_dwordx4 v234, s[32:35], s51 offen sc1 lds\n"
    "buffer_load_dwordx4 v235, s[32:35], s51 offen offset:1024 sc1 lds\n"
    "buffer_load_dwordx4 v236, s[32:35], s51 offen offset:2048 sc1 lds\n"
    "buffer_load_dwordx4 v237, s[32:35], s51 offen offset:3072 sc1 lds\n"
    "s_add_u32 m0, s61, 20480\n"
    "s_add_u32 s51, s51, s71\n"
    "buffer_load_dwordx4 v234, s[32:35], s51 offen sc1 lds\n"
    "buffer_load_dwordx4 v235, s[32:35], s51 offen offset:1024 sc1 lds\n"
    "buffer_load_dwordx4 v236, s[32:35], s51 offen offset:2048 sc1 lds\n"
    "buffer_load_dwordx4 v237, s[32:35], s51 offen offset:3072 sc1 lds\n"
    "v_readlane_b32 s50, v232, 26\n"
    "s_nop 0\n"
    "s_mov_b32 s51, s50\n"
    "s_add_u32 m0, s61, 32768\n"
    "s_waitcnt vmcnt(20)\n"
    "s_barrier\n"
    "buffer_load_dwordx4 v234, s[32:35], s51 offen sc1 lds\n"
    "buffer_load_dwordx4 v235, s[32:35], s51 offen offset:1024 sc1 lds\n"
    "buffer_load_dwordx4 v236, s[32:35], s51 offen offset:2048 sc1 lds\n"
    "buffer_load_dwordx4 v237, s[32:35], s51 offen offset:3072 sc1 lds\n"
    "s_add_u32 m0, s61, 36864\n"
    "s_add_u32 s51, s51, s71\n"
    "buffer_load_dwordx4 v234, s[32:35], s51 offen sc1 lds\n"
    "buffer_load_dwordx4 v235, s[32:35], s51 offen offset:1024 sc1 lds\n"
    "buffer_load_dwordx4 v236, s[32:35], s51 offen offset:2048 sc1 lds\n"
    "buffer_load_dwordx4 v237, s[32:35], s51 offen offset:3072 sc1 lds\n"
    "v_readlane_b32 s50, v232, 27\n"
    "s_nop 0\n"
    "s_mov_b32 s51, s50\n"
    "s_add_u32 m0, s61, 49152\n"
    "s_waitcnt vmcnt(16)\n"
    "s_barrier\n"
    "buffer_load_dwordx4 v234, s[32:35], s51 offen sc1 lds\n"
    "buffer_load_dwordx4 v235, s[32:35], s51 offen offset:1024 sc1 lds\n"
    "buffer_load_dwordx4 v236, s[32:35], s51 offen offset:2048 sc1 lds\n"
    "buffer_load_dwordx4 v237, s[32:35], s51 offen offset:3072 sc1 lds\n"
    "s_add_u32 m0, s61, 53248\n"
    "s_add_u32 s51, s51, s71\n"
    "buffer_load_dwordx4 v234, s[32:35], s51 offen sc1 lds\n"
    "buffer_load_dwordx4 v235, s[32:35], s51 offen offset:1024 sc1 lds\n"
    "buffer_load_dwordx4 v236, s[32:35], s51 offen offset:2048 sc1 lds\n"
    "buffer_load_dwordx4 v237, s[32:35], s51 offen offset:3072 sc1 lds\n"
    "s_waitcnt vmcnt(16)\n"
    "s_barrier\n"
    "s_add_u32 m0, s57, 65536\n"
    "s_nop 0\n"
    "buffer_load_dwordx4 v164, s[28:31], s59 offen lds\n"
    "buffer_load_dwordx4 v165, s[28:31], s59 offen offset:1024 lds\n"
    "buffer_load_dwordx4 v166, s[28:31], s59 offen offset:2048 lds\n"
    "buffer_load_dwordx4 v167, s[28:31], s59 offen offset:3072 lds\n"
    "s_waitcnt vmcnt(12)\n"
    "s_barrier\n"
    "s_waitcnt vmcnt(4)\n"
    "s_barrier\n"
    "s_waitcnt vmcnt(0)\n"
    "s_barrier\n"
    "s_endpgm\n"
    "Ls29_%=:\n"
    "s_sleep 1\n"
    "s_sub_u32 s47, s47, 1\n"
    "s_cbranch_scc1 Lexit_%=\n"
    "s_branch Lw28_%=\n"
    "Ls36_%=:\n"
    "s_sleep 2\n"
    "s_sub_u32 s47, s47, 1\n"
    "s_cbranch_scc1 Lexit_%=\n"
    "s_branch Lg35_%=\n"
    "Lh33_%=:\n"
    "Lg35_%=:\n"
    "buffer_load_dword v169, v168, s[24:27], s53 offen sc1\n"
    "s_waitcnt vmcnt(0)\n"
    "v_cmp_gt_u32 vcc, 1, v169\n"
    "s_cbranch_vccnz Ls36_%=\n"
    "s_branch Lb34_%=\n"
    "Ls38_%=:\n"
    "s_sleep 1\n"
    "s_sub_u32 s47, s47, 1\n"
    "s_cbranch_scc1 Lexit_%=\n"
    "s_branch Lw37_%=\n"
    "Ls41_%=:\n"
    "s_sleep 1\n"
    "s_sub_u32 s47, s47, 1\n"
    "s_cbranch_scc1 Lexit_%=\n"
    "s_branch Lw40_%=\n"
    "Ls45_%=:\n"
    "s_sleep 2\n"
    "s_sub_u32 s47, s47, 1\n"
    "s_cbranch_scc1 Lexit_%=\n"
    "s_branch Lg44_%=\n"
    "Lh42_%=:\n"
    "Lg44_%=:\n"
    "buffer_load_dword v250, v249, s[24:27], s44 offen sc1\n"
    "s_waitcnt vmcnt(0)\n"
    "v_cmp_gt_u32 vcc, 2, v250\n"
    "s_cbranch_vccnz Ls45_%=\n"
    "s_branch Lb43_%=\n"
    "Ls48_%=:\n"
    "s_sleep 5\n"
    "s_sub_u32 s47, s47, 1\n"
    "s_cbranch_scc1 Lexit_%=\n"
    "s_branch Lw47_%=\n"
    "Ls52_%=:\n"
    "s_sleep 2\n"
    "s_sub_u32 s47, s47, 1\n"
    "s_cbranch_scc1 Lexit_%=\n"
    "s_branch Lg51_%=\n"
    "Lh49_%=:\n"
    "Lg51_%=:\n"
    "buffer_load_dword v252, v253, s[24:27], 0 offen sc1\n"
    "s_waitcnt vmcnt(0)\n"
    "v_cmp_gt_u32 vcc, 1, v252\n"
    "s_cbranch_vccnz Ls52_%=\n"
    "s_branch Lb50_%=\n"
    "Lexit_%=:\n"
    "s_endpgm\n"
    "Lend_%=:\n"
    :
    : "{s[0:1]}"(__builtin_amdgcn_kernarg_segment_ptr()), "{s2}"(__builtin_amdgcn_workgroup_id_x()), "{v0}"(__builtin_amdgcn_workitem_id_x())
    : "memory", "vcc", "scc", "v1","v2","v3","v4","v5","v6","v7","v8","v9","v10","v11","v12","v13","v14","v15","v16","v17","v18","v19","v20","v21","v22","v23","v24","v25","v26","v27","v28","v29","v30","v31","v32","v33","v34","v35","v36","v37","v38","v39","v40","v41","v42","v43","v44","v45","v46","v47","v48","v49","v50","v51","v52","v53","v54","v55","v56","v57","v58","v59","v60","v61","v62","v63","v64","v65","v66","v67","v68","v69","v70","v71","v72","v73","v74","v75","v76","v77","v78","v79","v80","v81","v82","v83","v84","v85","v86","v87","v88","v89","v90","v91","v92","v93","v94","v95","v96","v97","v98","v99","v100","v101","v102","v103","v104","v105","v106","v107","v108","v109","v110","v111","v112","v113","v114","v115","v116","v117","v118","v119","v120","v121","v122","v123","v124","v125","v126","v127","v128","v129","v130","v131","v132","v133","v134","v135","v136","v137","v138","v139","v140","v141","v142","v143","v144","v145","v146","v147","v148","v149","v150","v151","v152","v153","v154","v155","v156","v157","v158","v159","v160","v161","v162","v163","v164","v165","v166","v167","v168","v169","v170","v171","v172","v173","v174","v175","v176","v177","v178","v179","v180","v181","v182","v183","v184","v185","v186","v187","v188","v189","v190","v191","v192","v193","v194","v195","v196","v197","v198","v199","v200","v201","v202","v203","v204","v205","v206","v207","v208","v209","v210","v211","v212","v213","v214","v215","v216","v217","v218","v219","v220","v221","v222","v223","v224","v225","v226","v227","v228","v229","v230","v231","v232","v233","v234","v235","v236","v237","v238","v239","v240","v241","v242","v243","v244","v245","v246","v247","v248","v249","v250","v251","v252","v253","v254","v255","s3","s4","s5","s6","s7","s8","s9","s10","s11","s12","s13","s14","s15","s16","s17","s18","s19","s20","s21","s22","s23","s24","s25","s26","s27","s28","s29","s30","s31","s33","s34","s35","s36","s37","s38","s39","s40","s41","s42","s43","s44","s45","s46","s47","s48","s49","s50","s51","s52","s53","s54","s55","s56","s57","s58","s59","s60","s61","s62","s63","s64","s65","s66","s67","s68","s69","s70","s71","s72","s73","s74","s75","s76","s77","s78","s79","s80","s81","s82","s83","s84","s85","s86","s87","s88","s89","s90","s91","s92","s93","s94","s95","s96","s97","s98","s99");
#endif
}

extern "C" void kernel_launch(void* const* d_in, const int* in_sizes, int n_in, void* d_out, int out_size, void* d_ws, size_t ws_size, hipStream_t stream) {
  (void)hipFuncSetAttribute((const void*)k_fused, hipFuncAttributeMaxDynamicSharedMemorySize, 163840);
  k_fused<<<256, 512, 163840, stream>>>(d_in[0], d_in[1], d_in[2], d_in[3], d_in[4], d_in[5], d_in[7], d_in[8], d_ws, d_out);
}
```
